```python
import torch

def _softmax(x, axis=-1):
    return torch.softmax(x, dim=axis)
def _gelu(x):
    return torch.nn.functional.gelu(x, approximate="tanh")
def _silu(x):
    return torch.nn.functional.silu(x)
def _sigmoid(x):
    return torch.sigmoid(x)
def _layernorm(x, axis=-1, eps=1e-5):
    m = x.mean(dim=axis, keepdim=True)
    v = x.var(dim=axis, keepdim=True, unbiased=False)
    return (x - m) / torch.sqrt(v + eps)
def _scatter_add(tgt, idx, src, axis=0):
    return tgt.clone().index_add_(axis, idx, src)
def _segment_sum(src, seg, n):
    out = torch.zeros((n,) + src.shape[1:], dtype=src.dtype)
    return out.index_add_(0, seg, src)
def _pad(x, pw):
    flat = tuple(v for pair in reversed(pw) for v in pair)
    return torch.nn.functional.pad(x, flat)

kernel_name = 'gated_mix__grouped_kv_mqa__ssm_conv_s4__prompt'

def setup_inputs():

    _w = (lambda a: (lambda t: t.to(torch.bfloat16) if t.is_floating_point() else t)(torch.as_tensor(a)))
    def _A_setup():
        B, S, D, H = 1, 2048, 448, 16
        Dh = D // H
        return {
            "x": _w((lambda *s: torch.randn(*s, dtype=torch.bfloat16) * 0.05)(B, S, D)),
            "wq": _w((lambda *s: torch.randn(*s, dtype=torch.bfloat16) * 1.6)(D, H * Dh)),
            "wk": _w((lambda *s: torch.randn(*s, dtype=torch.bfloat16) * 1.6)(D, Dh)),
            "wv": _w((lambda *s: torch.randn(*s, dtype=torch.bfloat16) * 0.05)(D, Dh)),
            "wo": _w((lambda *s: torch.randn(*s, dtype=torch.bfloat16) * 0.05)(H * Dh, D)),
        }
    _A_in = _A_setup()

    def _B_setup():
        B, L, D, N = 1, 2048, 448, 256
        A_re = -(lambda _x: torch.exp(torch.as_tensor(_x)))((lambda *s: torch.randn(*s, dtype=torch.bfloat16) * 3.2)(D, N))
        A_im = torch.pi * (lambda *s: torch.randn(*s, dtype=torch.bfloat16) * 0.05)(D, N)
        B_re = (lambda *s: torch.randn(*s, dtype=torch.bfloat16) * 0.2)(D, N)
        B_im = (lambda *s: torch.randn(*s, dtype=torch.bfloat16) * 0.2)(D, N)
        C_re = (lambda *s: torch.randn(*s, dtype=torch.bfloat16) * 0.2)(D, N)
        C_im = (lambda *s: torch.randn(*s, dtype=torch.bfloat16) * 0.2)(D, N)
        log_dt = (lambda *s: torch.randn(*s, dtype=torch.bfloat16) * 0.05)(D) * 0.5 - 4.0
        Dskip = (lambda *s: torch.randn(*s, dtype=torch.bfloat16) * 0.1)(D)
        return {
            "u": _w((lambda *s: torch.randn(*s, dtype=torch.bfloat16) * 0.05)(B, L, D)),
            "A_re": _w(A_re),
            "A_im": _w(A_im),
            "B_re": _w(B_re),
            "B_im": _w(B_im),
            "C_re": _w(C_re),
            "C_im": _w(C_im),
            "log_dt": _w(log_dt),
            "Dskip": _w(Dskip),
        }
    _B_in = _B_setup()

    return {
        "A_x": _A_in["x"],
        "A_wq": _A_in["wq"],
        "A_wk": _A_in["wk"],
        "A_wv": _A_in["wv"],
        "A_wo": _A_in["wo"],
        "B_u": _B_in["u"],
        "B_A_re": _B_in["A_re"],
        "B_A_im": _B_in["A_im"],
        "B_B_re": _B_in["B_re"],
        "B_B_im": _B_in["B_im"],
        "B_C_re": _B_in["C_re"],
        "B_C_im": _B_in["C_im"],
        "B_log_dt": _B_in["log_dt"],
        "B_Dskip": _B_in["Dskip"],
        "gate_w": _w((lambda *s: torch.randn(*s, dtype=torch.bfloat16) * 0.05)(448) * 16.0),
    }

def reference(A_x, A_wq, A_wk, A_wv, A_wo, B_u, B_A_re, B_A_im, B_B_re, B_B_im, B_C_re, B_C_im, B_log_dt, B_Dskip, gate_w):
    def _A_ref(x, wq, wk, wv, wo):
        B, S, D = x.shape
        H = 16
        Dh = D // H
        q = (lambda x, *d: x.permute(*d))((x @ wq).reshape(B, S, H, Dh), 0, 2, 1, 3)
        k = x @ wk
        v = x @ wv
        scores = torch.einsum("bhqd,bkd->bhqk", q, k) * (Dh ** -0.5)
        p = _softmax(scores, axis=-1)
        o = torch.einsum("bhqk,bkd->bhqd", p, v)
        return (lambda x, *d: x.permute(*d))(o, 0, 2, 1, 3).reshape(B, S, H * Dh) @ wo

    def _B_ref(u, A_re, A_im, B_re, B_im, C_re, C_im, log_dt, Dskip):
        Bsz, L, D = u.shape
        dt = torch.exp(log_dt)[:, None]
        A = A_re + 1j * A_im
        Bc = B_re + 1j * B_im
        Cc = C_re + 1j * C_im
        logdA = dt * A
        dB = dt * Bc
        l = (lambda x, dt: x.to(dt))(torch.arange(L), A_re.dtype)
        P = torch.exp(logdA[:, :, None] * l[None, None, :])
        K = torch.einsum("dn,dnl,dn->dl", Cc, P, dB).real
        L2 = 2 * L
        Kp = _pad(K, ((0, 0), (0, L)))
        up = (lambda x, *d: x.permute(*d))(u, 0, 2, 1)
        up = _pad(up, ((0, 0), (0, 0), (0, L)))
        Kf = (lambda x, axes=(-2,-1): torch.fft.rfft2(x.float(), dim=axes))(Kp[:, None, :], axes=(-2, -1))
        Uf = (lambda x, axes=(-2,-1): torch.fft.rfft2(x.float(), dim=axes))(up[:, :, None, :], axes=(-2, -1))
        Yf = Uf * Kf[None, :, :, :]
        y = (lambda x, s, axes=(-2,-1): torch.fft.irfft2(x, s=s, dim=axes).to(torch.bfloat16))(Yf, (1, L2), axes=(-2, -1))[:, :, 0, :L]
        y = (lambda x, *d: x.permute(*d))(y, 0, 2, 1) + u * Dskip[None, None, :]
        return y

    x = A_x
    g = _sigmoid((x * gate_w).sum(-1, keepdims=True))
    a = _A_ref(x, A_wq, A_wk, A_wv, A_wo)
    b = _B_ref(x, B_A_re, B_A_im, B_B_re, B_B_im, B_C_re, B_C_im, B_log_dt, B_Dskip)
    return g * a + (1 - g) * b
```

```cpp
#include <hip/hip_runtime.h>
#include <hip/hip_bf16.h>
#include <stdint.h>
typedef unsigned short u16;
constexpr int S = 2048, D = 448, H = 16, DH = 28, N = 256;
__device__ inline float bf2f(u16 b) { return __uint_as_float(((unsigned)b) << 16); }
__device__ inline u16 f2bf(float f) { unsigned u = __float_as_uint(f); u += 0x7FFF + ((u >> 16) & 1); return (u16)(u >> 16); }
__device__ inline float rbf(float f) { return bf2f(f2bf(f)); }

__global__ void n_proj(const u16* x, const u16* wq, const u16* wk, const u16* wv, float* q, float* k, float* v) {
  int i = blockIdx.x * blockDim.x + threadIdx.x; if (i >= S * 504) return;
  int t = i / 504, c = i % 504; const u16* w; int ld, cc; float* o;
  if (c < 448) { w = wq; ld = 448; cc = c; o = q + t * 448 + c; }
  else if (c < 476) { w = wk; ld = 28; cc = c - 448; o = k + t * 28 + cc; }
  else { w = wv; ld = 28; cc = c - 476; o = v + t * 28 + cc; }
  float a = 0; for (int j = 0; j < D; ++j) a += bf2f(x[t * D + j]) * bf2f(w[j * ld + cc]);
  *o = rbf(a);
}
__global__ void n_attn(const float* q, const float* k, const float* v, float* o) {
  int i = blockIdx.x * blockDim.x + threadIdx.x; if (i >= S * H) return;
  int t = i / H, h = i % H; float qq[DH], acc[DH]; for (int d = 0; d < DH; ++d) { qq[d] = q[t * D + h * DH + d]; acc[d] = 0; }
  float m = -1e30f, l = 0; const float sc = 0.18898223650461362f;
  for (int j = 0; j < S; ++j) { float s = 0; for (int d = 0; d < DH; ++d) s += qq[d] * k[j * DH + d]; s *= sc;
    if (s > m) { float f = __expf(m - s); l *= f; for (int d = 0; d < DH; ++d) acc[d] *= f; m = s; }
    float p = __expf(s - m); l += p; for (int d = 0; d < DH; ++d) acc[d] += p * v[j * DH + d]; }
  for (int d = 0; d < DH; ++d) o[t * D + h * DH + d] = rbf(acc[d] / l);
}
__global__ void n_kgen(const u16* Are, const u16* Aim, const u16* Bre, const u16* Bim, const u16* Cre, const u16* Cim, const u16* logdt, float* K) {
  int i = blockIdx.x * blockDim.x + threadIdx.x; if (i >= D * S) return;
  int d = i / S, l = i % S; float lf = rbf((float)l); float dt = rbf(expf(bf2f(logdt[d]))); float acc = 0;
  for (int n = 0; n < N; ++n) { int j = d * N + n; float ar = dt * bf2f(Are[j]), ai = dt * bf2f(Aim[j]); float br = dt * bf2f(Bre[j]), bi = dt * bf2f(Bim[j]); float cr = bf2f(Cre[j]), ci = bf2f(Cim[j]);
    float e = expf(ar * lf), sn, cs; sincosf(ai * lf, &sn, &cs); float pr = e * cs, pi = e * sn; float wr = cr * br - ci * bi, wi = cr * bi + ci * br; acc += wr * pr - wi * pi; }
  K[i] = acc;
}
__global__ void n_conv(const u16* x, const float* K, float* y) {
  int i = blockIdx.x * blockDim.x + threadIdx.x; if (i >= D * S) return;
  int t = i / D, d = i % D; float a = 0; for (int l = 0; l <= t; ++l) a += K[d * S + l] * bf2f(x[(t - l) * D + d]);
  y[i] = rbf(a);
}
__global__ void n_final(const u16* x, const float* o, const u16* wo, const float* y, const u16* dsk, const u16* gw, u16* out) {
  int i = blockIdx.x * blockDim.x + threadIdx.x; if (i >= D * S) return;
  int t = i / D, d = i % D; float gs = 0; for (int j = 0; j < D; ++j) gs += rbf(bf2f(x[t * D + j]) * bf2f(gw[j]));
  gs = rbf(gs); float g = rbf(1.f / (1.f + expf(-gs)));
  float a = 0; for (int j = 0; j < D; ++j) a += o[t * D + j] * bf2f(wo[j * D + d]); a = rbf(a);
  float b = rbf(y[i] + rbf(bf2f(x[i]) * bf2f(dsk[d])));
  out[i] = f2bf(rbf(g * a) + rbf(rbf(1.f - g) * b));
}
__global__ void n_empty(unsigned* p) { if (threadIdx.x == 0 && blockIdx.x == 0) p[0] = 1; }
#define EXTRA 0
extern "C" void kernel_launch(void* const* d_in, const int* in_sizes, int n_in, void* d_out, int out_size, void* d_ws, size_t ws_size, hipStream_t stream) {
  const u16* x = (const u16*)d_in[0]; const u16* wq = (const u16*)d_in[1]; const u16* wk = (const u16*)d_in[2]; const u16* wv = (const u16*)d_in[3]; const u16* wo = (const u16*)d_in[4];
  float* ws = (float*)d_ws; float* q = ws; float* k = q + S * D; float* v = k + S * DH; float* o = v + S * DH; float* K = o + S * D; float* y = K + S * D; unsigned* e = (unsigned*)(y + S * D);
  n_proj<<<(S * 504 + 255) / 256, 256, 0, stream>>>(x, wq, wk, wv, q, k, v);
  n_attn<<<S * H / 64, 64, 0, stream>>>(q, k, v, o);
  n_kgen<<<S * D / 256, 256, 0, stream>>>((const u16*)d_in[6], (const u16*)d_in[7], (const u16*)d_in[8], (const u16*)d_in[9], (const u16*)d_in[10], (const u16*)d_in[11], (const u16*)d_in[12], K);
  n_conv<<<S * D / 256, 256, 0, stream>>>(x, K, y);
  n_final<<<S * D / 256, 256, 0, stream>>>(x, o, wo, y, (const u16*)d_in[13], (const u16*)d_in[14], (u16*)d_out);
  for (int i = 0; i < EXTRA; ++i) n_empty<<<256, 512, 0, stream>>>(e);
}
```

```cpp
#include <hip/hip_runtime.h>
#include <hip/hip_bf16.h>
#include <stdint.h>

typedef unsigned short u16;
typedef unsigned long long u64;
using f32x2 = __attribute__((ext_vector_type(2))) float;
using f32x4 = __attribute__((ext_vector_type(4))) float;
using f32x16 = __attribute__((ext_vector_type(16))) float;
using bf16x2 = __attribute__((ext_vector_type(2))) __bf16;
using bf16x8 = __attribute__((ext_vector_type(8))) __bf16;
using s16x4 = __attribute__((ext_vector_type(4))) short;
using u32x2 = __attribute__((ext_vector_type(2))) unsigned;
using u32x4 = __attribute__((ext_vector_type(4))) unsigned;
typedef __amdgpu_buffer_rsrc_t rsrc_t;
#define LAS __attribute__((address_space(3)))
#define GAS __attribute__((address_space(1)))
#define DI __device__ __forceinline__
typedef LAS char* lptr;

constexpr int S = 2048, D = 448;
constexpr int WS_E = 0, WS_F0 = 4096, WS_F1 = WS_F0 + 16384, WS_F2 = WS_F1 + 16384, WS_G = WS_F2 + 16384;
constexpr int WS_KF = 65536, WS_VF = WS_KF + 131072, WS_Q = WS_VF + 131072, WS_XT = WS_Q + S * D * 2, WS_YT = WS_XT + S * D * 2;
constexpr int WS_OB = WS_YT + S * D * 2, WS_END = WS_OB + 2097152;
constexpr int L_X = 0, L_W = 57344, L_WV = L_W + 25600, L_SCR = 114688, SCR_W = 4352, TROW = 272;
constexpr int L_AC = 114688, AC_COPY = 4160, AC_CH = 4 * AC_COPY, L_U = 149504, U_G = 1280, U_CH = 4 * U_G, L_TS = 159744, L_MISC = 162816;
constexpr int L_A = 0, L_K0 = 65536, L_QS = 69632, L_B = 77824, L_C = 143360, L_LS = L_C + 16384;
constexpr int L_WO = 0, L_OS = 77824, L_WO3 = 110592, L_YT = 136192;
constexpr int LDS_BYTES = 163840;
constexpr int M_KMAX = L_MISC;
constexpr unsigned SENT = 0xFFFFFFFFu;
constexpr int SC1 = 16;
constexpr unsigned SPIN_CAP = 1u << 15;
#define CODE_PF_KB 0

struct Params {
  const u16 *x, *wq, *wk, *wv, *wo, *are, *aim, *bre, *bim, *cre, *cim, *logdt, *dsk, *gw;
  u16* out;
  char* ws;
};

#define MFMA32(a, b, c) __builtin_amdgcn_mfma_f32_32x32x16_bf16((a), (b), (c), 0, 0, 0)
#define MFMA16(a, b, c) __builtin_amdgcn_mfma_f32_16x16x32_bf16((a), (b), (c), 0, 0, 0)
#define SBAR() __builtin_amdgcn_sched_barrier(0)
#define PIN(x) asm volatile("" : "+v"(x))
#define MEMFENCE() asm volatile("" ::: "memory")
#define BAR_LDS() asm volatile("s_waitcnt lgkmcnt(0)\n\ts_barrier" ::: "memory")
#define BAR_ALL() asm volatile("s_waitcnt vmcnt(0) lgkmcnt(0)\n\ts_barrier" ::: "memory")
#define DRAIN() asm volatile("s_waitcnt vmcnt(0)" ::: "memory")

#define STAMP(i) do {} while (0)

DI float bf2f(unsigned b) { return __uint_as_float(b << 16); }
DI float bflo(unsigned w) { return __uint_as_float(w << 16); }
DI float bfhi(unsigned w) { return __uint_as_float(w & 0xFFFF0000u); }
DI unsigned cvtpk(float a, float b) { f32x2 v = {a, b}; return __builtin_bit_cast(unsigned, __builtin_convertvector(v, bf16x2)); }
DI float rbf(float a) { return bflo(cvtpk(a, a) & 0xFFFFu); }
DI bf16x8 asfrag(u32x4 v) { return __builtin_bit_cast(bf16x8, v); }
DI u32x4 ld128(lptr p) { return *(const LAS u32x4*)p; }
DI u32x2 ld64(lptr p) { return *(const LAS u32x2*)p; }
DI void st128(lptr p, u32x4 v) { *(LAS u32x4*)p = v; }
DI u32x2 trd(lptr p) { return __builtin_bit_cast(u32x2, __builtin_amdgcn_ds_read_tr16_b64_v4i16((LAS s16x4*)p)); }
DI u32x4 cat(u32x2 a, u32x2 b) { return (u32x4){a[0], a[1], b[0], b[1]}; }
DI rsrc_t mkrs(const void* p, int bytes) { return __builtin_amdgcn_make_buffer_rsrc((void*)p, (short)0, bytes, 0x00020000); }
DI float dot2(unsigned a, unsigned b, float c) { return __builtin_amdgcn_fdot2_f32_bf16(__builtin_bit_cast(bf16x2, a), __builtin_bit_cast(bf16x2, b), c, false); }
DI float swap32sum(float v) { auto r = __builtin_amdgcn_permlane32_swap(__float_as_uint(v), __float_as_uint(v), false, false); return __uint_as_float(r[0]) + __uint_as_float(r[1]); }
DI float swap32max(float v) { auto r = __builtin_amdgcn_permlane32_swap(__float_as_uint(v), __float_as_uint(v), false, false); return fmaxf(__uint_as_float(r[0]), __uint_as_float(r[1])); }
DI void dma(rsrc_t rs, unsigned voff, unsigned soff, unsigned dst) {
  asm volatile("s_mov_b32 m0, %0\n\ts_nop 0\n\tbuffer_load_dwordx4 %1, %2, %3 offen lds" :: "s"(dst), "v"(voff), "s"(rs), "s"(soff) : "memory");
}
DI void dma_sc1(rsrc_t rs, unsigned voff, unsigned soff, unsigned dst) {
  asm volatile("s_mov_b32 m0, %0\n\ts_nop 0\n\tbuffer_load_dwordx4 %1, %2, %3 offen sc1 lds" :: "s"(dst), "v"(voff), "s"(rs), "s"(soff) : "memory");
}
DI unsigned ldu16(const void* base, unsigned voff) { unsigned r; asm volatile("global_load_ushort %0, %1, %2" : "=v"(r) : "v"(voff), "s"(base) : "memory"); return r; }
template <int CTRL> DI unsigned dppmax(unsigned v) { return max(v, (unsigned)__builtin_amdgcn_update_dpp(0, (int)v, CTRL, 0xF, 0xF, true)); }
DI unsigned rowmax16(unsigned v) { v = dppmax<0xB1>(v); v = dppmax<0x4E>(v); v = dppmax<0x141>(v); return dppmax<0x140>(v); }
DI f32x2 cmul(f32x2 x, f32x2 z) {
  f32x2 t = x * (f32x2){z[0], z[0]};
  return __builtin_elementwise_fma((f32x2){x[1], x[0]}, (f32x2){-z[1], z[1]}, t);
}
DI u64 ldflag(const char* ws, int off) { return __hip_atomic_load((const GAS u64*)(ws + off), __ATOMIC_RELAXED, __HIP_MEMORY_SCOPE_AGENT); }
DI void stflag(char* ws, int off, u64 v) { __hip_atomic_store((GAS u64*)(ws + off), v, __ATOMIC_RELAXED, __HIP_MEMORY_SCOPE_AGENT); }
DI u64 waitflags(const char* ws, int off, u64 tag) {
  u64 v;
  for (unsigned spin = 0;; ++spin) {
    MEMFENCE();
    v = ldflag(ws, off);
    if (__all((v >> 16) == tag) || spin > SPIN_CAP) break;
    __builtin_amdgcn_s_sleep(1);
  }
  MEMFENCE();
  return v;
}

template <int WSTEP, int WHALF, bool SWAP>
DI f32x16 proj_loop(lptr wa, lptr xb0, lptr xb1, lptr xb2, lptr xb3) {
  constexpr int PD = 4;
  f32x16 acc = {};
  u32x4 wf[PD], xf[PD];
  auto rd = [&](int ks) {
    wf[ks % PD] = cat(trd(wa + ks * WSTEP), trd(wa + ks * WSTEP + WHALF));
    xf[ks % PD] = ld128(((ks & 3) == 0 ? xb0 : (ks & 3) == 1 ? xb1 : (ks & 3) == 2 ? xb2 : xb3) + (ks >> 2) * 128); };
#pragma unroll
  for (int ks = 0; ks < PD; ++ks) rd(ks);
  SBAR();
#pragma unroll
  for (int ks = 0; ks < 28; ++ks) {
    acc = SWAP ? MFMA32(asfrag(xf[ks % PD]), asfrag(wf[ks % PD]), acc) : MFMA32(asfrag(wf[ks % PD]), asfrag(xf[ks % PD]), acc);
    SBAR();
    if (ks + PD < 28) rd(ks + PD);
    SBAR();
  }
  return acc;
}

DI f32x16 attn_safe(rsrc_t rws, int lane, u32x4 q0, u32x4 q1) {
  if (lane >= 32) q1[2] = 0u;
  f32x16 o = {};
  float m = -INFINITY;
#pragma unroll 1
  for (int kb = 0; kb < 64; ++kb) {
    const u32x4 k0 = __builtin_amdgcn_raw_buffer_load_b128(rws, WS_KF + lane * 16, kb * 2048, SC1);
    const u32x4 k1 = __builtin_amdgcn_raw_buffer_load_b128(rws, WS_KF + 1024 + lane * 16, kb * 2048, SC1);
    const u32x4 v0 = __builtin_amdgcn_raw_buffer_load_b128(rws, WS_VF + lane * 16, kb * 2048, SC1);
    const u32x4 v1 = __builtin_amdgcn_raw_buffer_load_b128(rws, WS_VF + 1024 + lane * 16, kb * 2048, SC1);
    f32x16 s = {};
    s = MFMA32(asfrag(k0), asfrag(q0), s);
    s = MFMA32(asfrag(k1), asfrag(q1), s);
    float t = s[0];
#pragma unroll
    for (int i = 1; i < 16; ++i) t = fmaxf(t, s[i]);
    const float mn = fmaxf(m, swap32max(t));
    const float al = __builtin_amdgcn_exp2f(m - mn);
    m = mn;
    u32x4 p0, p1;
#pragma unroll
    for (int i = 0; i < 4; ++i) {
      p0[i] = cvtpk(__builtin_amdgcn_exp2f(s[2 * i] - mn), __builtin_amdgcn_exp2f(s[2 * i + 1] - mn));
      p1[i] = cvtpk(__builtin_amdgcn_exp2f(s[8 + 2 * i] - mn), __builtin_amdgcn_exp2f(s[9 + 2 * i] - mn));
    }
#pragma unroll
    for (int i = 0; i < 16; ++i) o[i] *= al;
    o = MFMA32(asfrag(v0), asfrag(p0), o);
    o = MFMA32(asfrag(v1), asfrag(p1), o);
  }
  return o;
}

struct Att {
  f32x16 sa, sb, o;
  u32x4 q0, q1, k0, k1, v0, v1, vp1, p0, p1;
};
#define EX(v) __builtin_amdgcn_exp2f(v)
template <int I, class F1, class F2>
DI void att_step(Att& a, f32x16& SC, f32x16& SN, lptr rdA, lptr rdB, F1 f1, F2 f2) {
  constexpr bool MORE = I < 31;
  constexpr int NS = I + 1;
  const lptr rd = ((NS >> 3) & 1) ? rdB + (NS & 7) * 8192 : rdA + (NS & 7) * 8192;
  u32x4 nk0, nk1, nv0, nv1;
  float e0, e1, e2, e3;
  const f32x16 zero = {};
  if constexpr (MORE) SN = MFMA32(asfrag(a.k0), asfrag(a.q0), zero);
  SBAR();
  if constexpr (MORE) { if constexpr (I < 30) nk0 = ld128(rd); nv0 = ld128(rd + 2048); }
  e0 = EX(SC[0]); e1 = EX(SC[1]); e2 = EX(SC[2]); e3 = EX(SC[3]);
  a.p0[0] = cvtpk(e0, e1); a.p0[1] = cvtpk(e2, e3);
  PIN(a.p0); SBAR();
  if constexpr (I > 0) a.o = MFMA32(asfrag(a.vp1), asfrag(a.p1), a.o);
  SBAR();
  f1();
  e0 = EX(SC[4]); e1 = EX(SC[5]); e2 = EX(SC[6]); e3 = EX(SC[7]);
  a.p0[2] = cvtpk(e0, e1); a.p0[3] = cvtpk(e2, e3);
  PIN(a.p0); SBAR();
  if constexpr (MORE) SN = MFMA32(asfrag(a.k1), asfrag(a.q1), SN);
  SBAR();
  if constexpr (MORE) { if constexpr (I < 30) nk1 = ld128(rd + 1024); nv1 = ld128(rd + 3072); }
  e0 = EX(SC[8]); e1 = EX(SC[9]); e2 = EX(SC[10]); e3 = EX(SC[11]);
  a.p1[0] = cvtpk(e0, e1); a.p1[1] = cvtpk(e2, e3);
  PIN(a.p1); SBAR();
  a.o = MFMA32(asfrag(a.v0), asfrag(a.p0), a.o);
  SBAR();
  f2();
  e0 = EX(SC[12]); e1 = EX(SC[13]); e2 = EX(SC[14]); e3 = EX(SC[15]);
  a.p1[2] = cvtpk(e0, e1); a.p1[3] = cvtpk(e2, e3);
  PIN(a.p1); SBAR();
  a.vp1 = a.v1;
  if constexpr (MORE) { a.v0 = nv0; a.v1 = nv1; if constexpr (I < 30) { a.k0 = nk0; a.k1 = nk1; } }
}

__global__ void __launch_bounds__(512, 2) gated_mix_kernel(Params P) {
  __shared__ __attribute__((aligned(16))) char lds_raw[LDS_BYTES];
  const lptr lds = (lptr)lds_raw;
  const unsigned ldsb = (unsigned)(uintptr_t)lds;
  STAMP(0);
  const int tid = threadIdx.x, lane = tid & 63, w = __builtin_amdgcn_readfirstlane(tid >> 6);
  const int b = blockIdx.x, xcd = b & 7, j = b >> 3;
  const int rb = 4 * xcd + (j >> 3), ct = j & 7;
  const int qb = 8 * xcd + (j >> 2), hg = j & 3;
  const bool isS = ct < 7;
  const int cp = 7 * rb + ct;
  const int r31 = lane & 31, hf = lane >> 5, c15 = lane & 15, g4 = lane >> 4, tq = c15 >> 2, tp = lane & 3;
  const rsrc_t rws = mkrs(P.ws, WS_END), rx = mkrs(P.x, S * D * 2);

  asm volatile("" :: "s"(P.x), "s"(P.ws));
  unsigned pa, pb, pc, pd, pe, pf, pl;
  if (isS) {
    const unsigned po = (unsigned)(cp * 512 + tid) * 2u;
    pl = ldu16(P.logdt, (unsigned)(2 * cp + (w >> 2)) * 2u);
    pa = ldu16(P.are, po); pb = ldu16(P.aim, po); pc = ldu16(P.bre, po); pd = ldu16(P.bim, po); pe = ldu16(P.cre, po); pf = ldu16(P.cim, po);
  }
  u64 epoch_v;
  asm volatile("global_load_dwordx2 %0, %1, %2 sc1" : "=v"(epoch_v) : "v"(0u), "s"(P.ws) : "memory");
  if (lane < 56) {
#pragma unroll
    for (int ii = 0; ii < 8; ++ii) {
      const int r = 8 * w + ((ii + ct) & 7);
      dma(rx, (unsigned)((lane ^ ((r >> 1) & 7)) << 4), (unsigned)((64 * rb + r) * 896), ldsb + L_X + r * 896);
    }
  }
  if (isS) {
    const rsrc_t rw = mkrs(P.wq, D * D * 2);
    const unsigned wvoff = (unsigned)((56 * w + (lane >> 3)) * 896 + 128 * ct + (((lane & 7) ^ ((g4 & 1) << 2)) << 4));
#pragma unroll
    for (int ii = 0; ii < 7; ++ii) {
      const unsigned t = (unsigned)(ii + 2 * (rb & 3)), i = t - 7u * ((t * 37u) >> 8);
      dma(rw, wvoff, i * 7168u, ldsb + L_W + w * 7168 + i * 1024);
    }
  } else {
    const rsrc_t rwk = mkrs(P.wk, D * 28 * 2), rwv = mkrs(P.wv, D * 28 * 2);
#pragma unroll
    for (int ii = 0; ii < 4; ++ii) {
      const int p = 8 * ((ii + rb) & 3) + w;
      if (p < 25) { dma(rwk, (unsigned)(lane * 16), (unsigned)(p * 1024), ldsb + L_W + p * 1024); dma(rwv, (unsigned)(lane * 16), (unsigned)(p * 1024), ldsb + L_WV + p * 1024); }
    }
  }
  __builtin_amdgcn_raw_buffer_store_b128((u32x4){SENT, SENT, SENT, SENT}, rws, WS_OB + (qb * 4 + hg) * 8192 + tid * 16, 0, SC1);
  if (isS) {
    asm volatile("s_waitcnt vmcnt(17)" : "+v"(pa), "+v"(pb), "+v"(pc), "+v"(pd), "+v"(pe), "+v"(pf), "+v"(pl) :: "memory");
    STAMP(1);
    const lptr sc = lds + L_SCR + w * SCR_W;
    const lptr tw = sc + lane * 4, tr = sc + c15 * TROW + g4 * 16;
    const float dt = rbf(expf(bf2f(pl)));
    const float Lr = dt * bf2f(pa) * 1.4426950408889634f, Rv = dt * bf2f(pb) * 0.15915494309189535f;
    const float br = dt * bf2f(pc), bi = dt * bf2f(pd), cr = bf2f(pe), ci = bf2f(pf);
    const f32x2 W = {cr * br - ci * bi, cr * bi + ci * br};
    auto zexp = [&](float g) { const float e = EX(Lr * g), r = __builtin_amdgcn_fractf(Rv * g); return (f32x2){e * __builtin_amdgcn_cosf(r), e * __builtin_amdgcn_sinf(r)}; };
    auto put = [&](int row, f32x2 v) { *(LAS unsigned*)(tw + row * TROW) = cvtpk(v[0], v[1]); };
    const f32x2 z1 = zexp(1.f), Z = zexp(128.f);
    f32x2 z8 = zexp(8.f); z8[1] = -z8[1];
    const f32x2 z16 = cmul(z8, z8);
    f32x2 ea = {1.f, 0.f}, eb = z8;
#pragma unroll
    for (int e = 0; e < 16; e += 2) { put(e, ea); put(e + 1, eb); if (e < 14) { ea = e ? cmul(ea, z16) : z16; eb = cmul(eb, z16); } }
    MEMFENCE();
    u32x4 ef[4], gf[4];
#pragma unroll
    for (int ks = 0; ks < 4; ++ks) ef[ks] = ld128(tr + ks * 64);
    MEMFENCE();
    f32x2 pw[8]; pw[0] = W;
#pragma unroll
    for (int i = 1; i < 8; ++i) pw[i] = cmul(pw[i - 1], z1);
#pragma unroll
    for (int i = 0; i < 8; ++i) { put(i, pw[i]); put(8 + i, cmul(pw[i], Z)); }
    f32x4 kt[3];
    auto rdtile = [&]() { MEMFENCE();
#pragma unroll
      for (int ks = 0; ks < 4; ++ks) gf[ks] = ld128(tr + ks * 64);
      MEMFENCE(); };
    auto tile = [&](int t) { f32x4 a = {};
#pragma unroll
      for (int ks = 0; ks < 4; ++ks) a = MFMA16(asfrag(gf[ks]), asfrag(ef[ks]), a);
      kt[t] = a; };
    rdtile();
    {
      asm volatile("s_waitcnt vmcnt(8)" ::: "memory");
      const int r0 = 8 * w + tq, r1 = r0 + 4, ch = 2 * g4 + (tp >> 1);
      const u32x2 lo = trd(lds + L_X + r0 * 896 + ct * 128 + ((ch ^ ((r0 >> 1) & 7)) << 4) + (tp & 1) * 8);
      const u32x2 hi = trd(lds + L_X + r1 * 896 + ct * 128 + ((ch ^ ((r1 >> 1) & 7)) << 4) + (tp & 1) * 8);
      __builtin_amdgcn_raw_buffer_store_b128(cat(lo, hi), rws, WS_XT + ((8 * rb + w) * D + 64 * ct + lane) * 16, 0, SC1);
    }
    tile(0);
    f32x2 Zk = cmul(Z, Z);
#pragma unroll
    for (int i = 0; i < 4; ++i) put(i, cmul(pw[2 * i], Zk));
    Zk = cmul(Zk, Z);
#pragma unroll
    for (int i = 0; i < 4; ++i) put(4 + i, cmul(pw[2 * i], Zk));
    f32x2 last;
#pragma unroll
    for (int i = 0; i < 4; ++i) { Zk = cmul(Zk, Z); last = cmul(pw[0], Zk); put(8 + 2 * i, last); put(9 + 2 * i, cmul(pw[4], Zk)); }
    rdtile();
    tile(1);
#pragma unroll
    for (int i = 0; i < 9; ++i) { last = cmul(last, Z); put(i, last); }
    rdtile();
    tile(2);
#pragma unroll
    for (int t = 0; t < 3; ++t) st128(sc + t * 1024 + lane * 16, __builtin_bit_cast(u32x4, kt[t]));
  }
  STAMP(2);
  asm volatile("s_waitcnt vmcnt(0) lgkmcnt(0)\n\ts_barrier" : "+v"(epoch_v) :: "memory");
  STAMP(3);
  const u64 epoch = ((u64)__builtin_amdgcn_readfirstlane((unsigned)(epoch_v >> 32)) << 32) | (unsigned)__builtin_amdgcn_readfirstlane((unsigned)epoch_v);
  u64 tag;
  { u64 z = epoch + 0x9E3779B97F4A7C15ull; z = (z ^ (z >> 30)) * 0xBF58476D1CE4E5B9ull; z = (z ^ (z >> 27)) * 0x94D049BB133111EBull; tag = (z ^ (z >> 31)) >> 16; }
  if (tid == 256) stflag(P.ws, WS_F0 + (ct * 32 + rb) * 64, tag << 16);

  if (w < 4) {
    const int tt = w >> 1, row = 32 * tt + r31, f = ((row >> 1) & 7) ^ hf;
    const lptr xr = lds + L_X + row * 896;
    const lptr xb0 = xr + (f << 4), xb1 = xr + ((f ^ 2) << 4), xb2 = xr + ((f ^ 4) << 4), xb3 = xr + ((f ^ 6) << 4);
    if (isS) {
      const int mt = w & 1;
      const lptr wa = lds + L_W + (8 * hf + tq) * 128 + (((4 * mt + 2 * (g4 & 1) + (tp >> 1)) ^ ((tq >> 1) << 2)) << 4) + (tp & 1) * 8;
      const f32x16 acc = proj_loop<2048, 512, false>(wa, xb0, xb1, xb2, xb3);
      constexpr float QS = 0.18898223650461362f * 1.4426950408889634f;
#pragma unroll
      for (int i = 0; i < 4; ++i) {
        const u32x2 v = {cvtpk(acc[4 * i] * QS, acc[4 * i + 1] * QS), cvtpk(acc[4 * i + 2] * QS, acc[4 * i + 3] * QS)};
        __builtin_amdgcn_raw_buffer_store_b64(v, rws, WS_Q + ((16 * ct + 8 * mt + 2 * i + hf) * S + 64 * rb + row) * 8, 0, SC1);
      }
    } else {
      const lptr wa = lds + L_W + (8 * hf + tq) * 56 + 32 * (g4 & 1) + 8 * tp;
      u32x4 f0, f1;
      const int off = ((w & 1) ? WS_VF : WS_KF) + (2 * rb + tt) * 2048 + lane * 16;
      if (w & 1) {
        const f32x16 acc = proj_loop<896, 224, true>(wa + (L_WV - L_W), xb0, xb1, xb2, xb3);
#pragma unroll
        for (int i = 0; i < 4; ++i) { f0[i] = cvtpk(acc[2 * i], acc[2 * i + 1]); f1[i] = cvtpk(acc[8 + 2 * i], acc[9 + 2 * i]); }
        if (r31 >= 28) { const unsigned c = r31 == 28 ? 0x3F803F80u : 0u; f0 = (u32x4){c, c, c, c}; f1 = f0; }
        __builtin_amdgcn_raw_buffer_store_b128(f0, rws, off, 0, SC1);
        __builtin_amdgcn_raw_buffer_store_b128(f1, rws, off + 1024, 0, SC1);
      } else {
        const f32x16 acc = proj_loop<896, 224, false>(wa, xb0, xb1, xb2, xb3);
#pragma unroll
        for (int i = 0; i < 4; ++i) { f0[i] = cvtpk(acc[2 * i], acc[2 * i + 1]); f1[i] = cvtpk(acc[8 + 2 * i], acc[9 + 2 * i]); }
        if (hf) { f1[2] = 0x00003F80u; f1[3] = 0u; }
        __builtin_amdgcn_raw_buffer_store_b128(f0, rws, off, 0, SC1);
        __builtin_amdgcn_raw_buffer_store_b128(f1, rws, off + 1024, 0, SC1);
        float n2 = hf ? -1.f : 0.f;
#pragma unroll
        for (int i = 0; i < 4; ++i) { n2 = dot2(f0[i], f0[i], n2); n2 = dot2(f1[i], f1[i], n2); }
        const unsigned nm = rowmax16(__float_as_uint(fmaxf(swap32sum(n2), 0.f)));
        const unsigned nw = max((unsigned)__builtin_amdgcn_readlane((int)nm, 0), (unsigned)__builtin_amdgcn_readlane((int)nm, 16));
        if (lane == 0) *(LAS unsigned*)(lds + M_KMAX + tt * 4) = nw;
      }
    }
  } else {
    const int w4 = w - 4, t4 = tid - 256;
    if (isS) {
#pragma unroll
      for (int rr = 0; rr < 2; ++rr) {
        const int wi = w4 + 4 * rr;
        if (wi < 6) {
          const int ch = wi >= 3 ? 1 : 0, t = wi - 3 * ch;
          const lptr sp = lds + L_SCR + ch * 4 * SCR_W + t * 1024 + lane * 16;
          const f32x4 v = __builtin_bit_cast(f32x4, ld128(sp)) + __builtin_bit_cast(f32x4, ld128(sp + SCR_W)) + __builtin_bit_cast(f32x4, ld128(sp + 2 * SCR_W)) +
                          __builtin_bit_cast(f32x4, ld128(sp + 3 * SCR_W));
          int base, o1, o2;
          if (t == 0) { base = 128 * (g4 >> 1) + 4 * (g4 & 1) + 8 * c15; o1 = 1; o2 = 2; }
          else if (t == 1) { if (g4 < 2) { base = 256 + 64 * g4 + 4 * c15; o1 = 1; o2 = 2; } else { base = 384 + 64 * (g4 - 2) + 2 * c15; o1 = 1; o2 = 32; } }
          else { base = 512 + 64 * g4 + c15; o1 = 16; o2 = 32; }
          LAS u16* tsp = (LAS u16*)(lds + L_TS + ch * 1536) + base;
          const unsigned a = cvtpk(v[0], v[1]), c = cvtpk(v[2], v[3]);
          tsp[0] = (u16)a; tsp[o1] = (u16)(a >> 16); tsp[o2] = (u16)c; tsp[o1 + o2] = (u16)(c >> 16);
        }
      }
      if (t4 < 128 && c15 != 15) st128(lds + L_U + (t4 >> 4) * U_G + c15 * 16, (u32x4){0u, 0u, 0u, 0u});
      waitflags(P.ws, WS_F0 + ((cp >> 5) * 32 + r31) * 64, tag);
      const int m = t4 >> 1, uo = WS_XT + (m * D + 2 * cp + (t4 & 1)) * 16;
      const u32x4 u0 = __builtin_amdgcn_raw_buffer_load_b128(rws, uo, 0, SC1);
      const u32x4 u1 = __builtin_amdgcn_raw_buffer_load_b128(rws, uo, 128 * D * 16, SC1);
      const lptr dst = lds + L_U + (t4 & 1) * U_CH + (m & 3) * U_G + ((m >> 2) + 15) * 16;
      st128(dst, u0); st128(dst + 512, u1);
    } else {
      const int row = t4 >> 2, sub = t4 & 3, f = (row >> 1) & 7;
      u32x4 gv[14];
#pragma unroll
      for (int i = 0; i < 14; ++i) gv[i] = *(const GAS u32x4*)(P.gw + (sub + 4 * i) * 8);
      float s = 0.f;
#pragma unroll
      for (int i = 0; i < 14; ++i) {
        const int c = sub + 4 * i;
        const u32x4 xv = ld128(lds + L_X + row * 896 + (((c & ~7) | ((c & 7) ^ f)) << 4));
#pragma unroll
        for (int k = 0; k < 4; ++k) s = dot2(xv[k], gv[i][k], s);
      }
      s += __builtin_bit_cast(float, __builtin_amdgcn_update_dpp(0, __builtin_bit_cast(int, s), 0xB1, 0xF, 0xF, true));
      s += __builtin_bit_cast(float, __builtin_amdgcn_update_dpp(0, __builtin_bit_cast(int, s), 0x4E, 0xF, 0xF, true));
      const float gs = rbf(s), gt = rbf(1.f / (1.f + expf(-gs)));
      if (sub == 0) __builtin_amdgcn_raw_buffer_store_b32(__float_as_uint(gt), rws, WS_G + (64 * rb + row) * 4, 0, SC1);
    }
  }
  if (!isS) DRAIN();
  STAMP(4);
  BAR_LDS();
  STAMP(5);

  if (isS) {
    const int ch = w >> 2, m = tid & 255, l0 = 2040 - 8 * m;
    const LAS u16* T = (const LAS u16*)(lds + L_TS + ch * 1536);
    auto look = [&](int l, int lseg) {
      const int e = (lseg >= 256) + (lseg >= 512) + (lseg >= 1024);
      const int bias = ((1 << e) >> 1) - (e ? 1 : 0), bm = e ? 1 : 0;
      return (unsigned)T[128 * e + ((l + bias + ((l >> e) & bm)) >> e)]; };
    const int l1 = l0 ? l0 : 8;
    unsigned d[6];
#pragma unroll
    for (int k = 0; k < 4; ++k) d[k] = look(l0 + 7 - 2 * k, l0) | (look(l0 + 6 - 2 * k, l0) << 16);
    d[4] = look(l1 - 1, l1 - 8) | (look(l1 - 2, l1 - 8) << 16);
    d[5] = look(l1 - 3, l1 - 8);
    if (l0 == 0) { d[4] = 0u; d[5] = 0u; }
    unsigned al[5];
#pragma unroll
    for (int k = 0; k < 5; ++k) al[k] = __builtin_amdgcn_alignbit(d[k + 1], d[k], 16);
    const lptr dst = lds + L_AC + ch * AC_CH + m * 16;
    st128(dst, (u32x4){d[0], d[1], d[2], d[3]});
    st128(dst + AC_COPY, (u32x4){al[0], al[1], al[2], al[3]});
    st128(dst + 2 * AC_COPY, (u32x4){d[1], d[2], d[3], d[4]});
    st128(dst + 3 * AC_COPY, (u32x4){al[1], al[2], al[3], al[4]});
    if (tid < 32) st128(lds + L_AC + (tid >> 2) * AC_COPY + 4096 + (tid & 3) * 16, (u32x4){0u, 0u, 0u, 0u});
  } else if (tid == 0) {
    const u32x2 km = ld64(lds + M_KMAX);
    stflag(P.ws, WS_F1 + (ct * 32 + rb) * 64, (tag << 16) | (u64)((max(km[0], km[1]) >> 16) + 1u));
  }
  STAMP(6);
  BAR_ALL();
  STAMP(7);
  if (isS && tid == 256) stflag(P.ws, WS_F1 + (ct * 32 + rb) * 64, tag << 16);

  Att a;
  const int hh = w & 3, kh = w >> 2;
  const unsigned kvoff = (unsigned)(((w & 2) ? WS_VF : WS_KF + 2048) + (w & 1) * 1024 + lane * 16);
  const rsrc_t rwo = mkrs(P.wo, D * D * 2);
  if (w < 4) {
    if (isS) {
      const int ch = w >> 1, mt = w & 1, rp = 15 - c15;
      const lptr ab = lds + L_AC + ch * AC_CH + (rp & 3) * AC_COPY + 2 * (16 * (1 - mt) + (rp & 12) + 8 * g4);
      lptr ab2 = ab + 8; PIN(ab2);
      const lptr ub = lds + L_U + ch * U_CH + g4 * U_G + c15 * 16;
      f32x4 y[4] = {};
      u32x4 af[2][4], bfr[2][4];
      auto rda = [&](int d0, int i) { const int ao = 64 * (63 - d0 - 16 * i); af[d0 & 1][i] = cat(ld64(ab + ao), ld64(ab2 + ao)); };
      auto rdb = [&](int d0, int i) { bfr[d0 & 1][i] = ld128(ub + (15 - d0) * 16 + 256 * i); };
#pragma unroll
      for (int i = 0; i < 4; ++i) { rda(0, i); rdb(0, i); }
      SBAR();
#pragma unroll
      for (int d0 = 0; d0 < 16; ++d0) {
        const int c = d0 & 1;
        const bool more = d0 < 15;
#define CM(i, nt) y[nt] = MFMA16(asfrag(af[c][i]), asfrag(bfr[c][nt - i]), y[nt]); SBAR();
        CM(0, 0) if (more) rda(d0 + 1, 0); SBAR();
        CM(0, 1) if (more) rdb(d0 + 1, 0); SBAR();
        CM(0, 2) if (more) rdb(d0 + 1, 1); SBAR();
        CM(0, 3) if (more) rdb(d0 + 1, 2); SBAR();
        CM(1, 1) if (more) rdb(d0 + 1, 3); SBAR();
        CM(1, 2) if (more) rda(d0 + 1, 1); SBAR();
        CM(1, 3) if (more) rda(d0 + 1, 2); SBAR();
        CM(2, 2) if (more) rda(d0 + 1, 3); SBAR();
        CM(2, 3) CM(3, 3)
#undef CM
      }
#pragma unroll
      for (int nt = 0; nt < 4; ++nt) {
        const u32x2 v = {cvtpk(y[nt][0], y[nt][1]), cvtpk(y[nt][2], y[nt][3])};
        __builtin_amdgcn_raw_buffer_store_b64(v, rws, WS_YT + ((2 * cp + ch) * S + 32 * (16 * nt + c15) + 16 * mt + 4 * g4) * 2, 0, SC1);
      }
    }
    STAMP(8);
    BAR_LDS();
  } else {
#pragma unroll
    for (int i = 0; i < 4; ++i) dma(rwo, (unsigned)(lane * 16), (unsigned)(j * 12544 + min(4 * i + hh, 12) * 1024), ldsb + L_B + hh * 1024);
    const int c0 = (112 * hg) >> 6, c1 = (112 * hg + 111) >> 6;
    int fo = WS_F1 + (7 * 32 + r31) * 64;
    if (lane >= 32 && lane < 35) fo = WS_F1 + (min(c0 + lane - 32, c1) * 32 + rb) * 64;
    if (lane >= 35 && lane < 38) fo = WS_F0 + (((j & 4) | ((hg + lane - 34) & 3)) * 32 + rb) * 64;
    const u64 fl = waitflags(P.ws, fo, tag);
    const int qo = WS_Q + ((7 * (4 * hg + hh) + hf) * S + 32 * qb + r31) * 8;
    const u32x2 qa = __builtin_amdgcn_raw_buffer_load_b64(rws, qo, 0, SC1), qc = __builtin_amdgcn_raw_buffer_load_b64(rws, qo, 2 * S * 8, SC1);
    const u32x2 qd = __builtin_amdgcn_raw_buffer_load_b64(rws, qo, 4 * S * 8, SC1);
    u32x2 qe = __builtin_amdgcn_raw_buffer_load_b64(rws, qo, 6 * S * 8, SC1);
    dma_sc1(rws, (unsigned)(WS_KF + lane * 16), (unsigned)(j * 8192 + hh * 2048), ldsb + L_B + 4096 + hh * 1024);
    dma_sc1(rws, (unsigned)(WS_KF + lane * 16), (unsigned)(j * 8192 + hh * 2048 + 1024), ldsb + L_B + 4096 + hh * 1024);
    dma_sc1(rws, (unsigned)(WS_KF + lane * 16), (unsigned)((hh >> 1) * 65536 + (hh & 1) * 1024), ldsb + L_K0 + hh * 1024);
#pragma unroll
    for (int i = 0; i < 8; ++i) {
      dma_sc1(rws, kvoff, (unsigned)(i * 2048), ldsb + L_A + i * 8192 + hh * 1024);
      dma_sc1(rws, kvoff, (unsigned)(65536 + i * 2048), ldsb + L_A + i * 8192 + 4096 + hh * 1024);
    }
    const unsigned kr = rowmax16((unsigned)(fl & 0xFFFFu));
    const unsigned km = max((unsigned)__builtin_amdgcn_readlane((int)kr, 0), (unsigned)__builtin_amdgcn_readlane((int)kr, 16));
    if (hf) qe = (u32x2){0u, 0u};
    a.q0 = cat(qa, qc); a.q1 = cat(qd, qe);
    float n2 = 0.f;
#pragma unroll
    for (int i = 0; i < 4; ++i) { n2 = dot2(a.q0[i], a.q0[i], n2); n2 = dot2(a.q1[i], a.q1[i], n2); }
    n2 = swap32sum(n2);
    const float mb = __builtin_amdgcn_sqrtf(n2 * bf2f(km));
    if (hf) a.q1[2] = cvtpk(-mb, 0.f);
    st128(lds + L_QS + hh * 2048 + lane * 16, a.q0);
    st128(lds + L_QS + hh * 2048 + 1024 + lane * 16, a.q1);
    STAMP(8);
    BAR_ALL();
  }
  STAMP(9);

  if (w < 4) { a.q0 = ld128(lds + L_QS + hh * 2048 + lane * 16); a.q1 = ld128(lds + L_QS + hh * 2048 + 1024 + lane * 16); }
  const lptr rdA = lds + L_A + kh * 4096 + lane * 16, rdB = lds + L_B + kh * 4096 + lane * 16;
  {
    const u32x4 f0 = ld128(lds + L_K0 + kh * 2048 + lane * 16), f1 = ld128(lds + L_K0 + kh * 2048 + 1024 + lane * 16);
    a.k0 = ld128(rdA); a.k1 = ld128(rdA + 1024); a.v0 = ld128(rdA + 2048); a.v1 = ld128(rdA + 3072);
    const f32x16 zero = {};
    a.sa = MFMA32(asfrag(f0), asfrag(a.q0), zero);
    a.sa = MFMA32(asfrag(f1), asfrag(a.q1), a.sa);
    a.o = zero; a.vp1 = a.v1; a.p1 = (u32x4){0u, 0u, 0u, 0u}; a.p0 = a.p1;
  }
  const unsigned kvs = (unsigned)(kh * 65536);
  auto fill = [&](int stage, int i) { dma_sc1(rws, kvoff, kvs + (unsigned)((8 * stage + i) * 2048), ldsb + ((stage & 1) ? L_B : L_A) + i * 8192 + w * 1024); };
  const int wc = 64 * min(w, 6) + lane, wrow = (wc * 4682) >> 16;
  const unsigned wovoff = (unsigned)(wrow * 896 + 224 * hg + (wc - 14 * wrow) * 16);
  const int g0 = hg == 3 ? 1 : 0, glate = hg == 3 ? 0 : 3;
  auto fillwo = [&](int i) { if (w < (i < 10 ? 7 : 4)) dma(rwo, wovoff, (unsigned)((112 * g0 + 32 * i) * 896), ldsb + L_WO + (7 * i + w) * 1024); };
#define NOP [&]() {}
#define KV(s, i) [&]() { fill(s, i); }
#define WO(i) [&]() { fillwo(i); }
#define STEP(I, F1, F2) att_step<I>(a, (I & 1) ? a.sb : a.sa, (I & 1) ? a.sa : a.sb, rdA, rdB, F1, F2);
  SBAR();
  STEP(0, KV(1, 0), KV(1, 1)) STEP(1, KV(1, 2), KV(1, 3)) STEP(2, KV(1, 4), NOP) STEP(3, KV(1, 5), NOP) STEP(4, KV(1, 6), NOP) STEP(5, KV(1, 7), NOP) STEP(6, NOP, NOP)
  BAR_ALL(); SBAR();
  if (isS && tid == 0) stflag(P.ws, WS_F2 + cp * 64, tag << 16);
  SBAR();
  STEP(7, KV(2, 0), KV(2, 1)) STEP(8, KV(2, 2), NOP) STEP(9, KV(2, 3), NOP) STEP(10, KV(2, 4), NOP) STEP(11, KV(2, 5), NOP) STEP(12, KV(2, 6), NOP) STEP(13, KV(2, 7), NOP) STEP(14, NOP, NOP)
  BAR_ALL(); SBAR();
  const int w6 = min(w, 6);
  const int f2o = WS_F2 + (56 * hg + 8 * w6 + (lane & 7)) * 64;
  STEP(15, KV(3, 0), KV(3, 1)) STEP(16, KV(3, 2), NOP) STEP(17, KV(3, 3), NOP) STEP(18, KV(3, 4), NOP) STEP(19, KV(3, 5), NOP) STEP(20, KV(3, 6), NOP)
  u64 f2;
  STEP(21, KV(3, 7), [&]() { f2 = ldflag(P.ws, f2o); }) STEP(22, NOP, NOP)
  BAR_ALL(); SBAR();
  if (!__all((f2 >> 16) == tag)) waitflags(P.ws, f2o, tag);
  MEMFENCE();
  const int n0 = 112 * hg + 16 * w6;
  const u32x4 yreg = __builtin_amdgcn_raw_buffer_load_b128(rws, WS_YT + ((n0 + (lane >> 2)) * S + 32 * qb + 8 * tp) * 2, 0, SC1);
  const u32x2 xe0 = __builtin_amdgcn_raw_buffer_load_b64(rx, ((32 * qb + c15) * D + n0 + 4 * g4) * 2, 0, 0);
  const u32x2 xe1 = __builtin_amdgcn_raw_buffer_load_b64(rx, ((32 * qb + 16 + c15) * D + n0 + 4 * g4) * 2, 0, 0);
  const u32x2 dk = *(const GAS u32x2*)(P.dsk + n0 + 4 * g4);
  const unsigned gt0 = __builtin_amdgcn_raw_buffer_load_b32(rws, WS_G + (32 * qb + c15) * 4, 0, SC1);
  const unsigned gt1 = __builtin_amdgcn_raw_buffer_load_b32(rws, WS_G + (32 * qb + 16 + c15) * 4, 0, SC1);
  MEMFENCE(); SBAR();
  STEP(23, WO(0), WO(1)) STEP(24, WO(2), WO(3)) STEP(25, WO(4), WO(5)) STEP(26, WO(6), WO(7)) STEP(27, WO(8), NOP) STEP(28, WO(9), NOP) STEP(29, WO(10), NOP) STEP(30, NOP, NOP) STEP(31, NOP, NOP)
  a.o = MFMA32(asfrag(a.vp1), asfrag(a.p1), a.o);
  STAMP(10);

  {
    const lptr xo = lds + L_C + w * 2048 + lane * 16;
    if (kh == 0) { st128(xo, __builtin_bit_cast(u32x4, (f32x4){a.o[8], a.o[9], a.o[10], a.o[11]})); st128(xo + 1024, __builtin_bit_cast(u32x4, (f32x4){a.o[12], a.o[13], a.o[14], a.o[15]})); }
    else { st128(xo, __builtin_bit_cast(u32x4, (f32x4){a.o[0], a.o[1], a.o[2], a.o[3]})); st128(xo + 1024, __builtin_bit_cast(u32x4, (f32x4){a.o[4], a.o[5], a.o[6], a.o[7]})); }
    if (hf) *(LAS float*)(lds + L_LS + w * 128 + r31 * 4) = a.o[12];
  }
  BAR_ALL();
  {
    const lptr xi = lds + L_C + (w ^ 4) * 2048 + lane * 16;
    f32x4 o0 = __builtin_bit_cast(f32x4, ld128(xi)), o1 = __builtin_bit_cast(f32x4, ld128(xi + 1024));
    float l = *(const LAS float*)(lds + L_LS + hh * 128 + r31 * 4) + *(const LAS float*)(lds + L_LS + (hh + 4) * 128 + r31 * 4);
    if (kh == 0) { o0 += (f32x4){a.o[0], a.o[1], a.o[2], a.o[3]}; o1 += (f32x4){a.o[4], a.o[5], a.o[6], a.o[7]}; }
    else { o0 += (f32x4){a.o[8], a.o[9], a.o[10], a.o[11]}; o1 += (f32x4){a.o[12], a.o[13], a.o[14], a.o[15]}; }
    if (__builtin_expect(!__all(l >= 0x1p-80f && l <= 0x1p80f), 0)) {
      const f32x16 so = attn_safe(rws, lane, a.q0, a.q1);
      l = swap32sum(hf ? so[12] : 0.f);
      if (kh == 0) { o0 = (f32x4){so[0], so[1], so[2], so[3]}; o1 = (f32x4){so[4], so[5], so[6], so[7]}; }
      else { o0 = (f32x4){so[8], so[9], so[10], so[11]}; o1 = (f32x4){so[12], so[13], so[14], so[15]}; }
    }
    const float inv = __builtin_amdgcn_rcpf(l);
    u32x4 of = {cvtpk(o0[0] * inv, o0[1] * inv), cvtpk(o0[2] * inv, o0[3] * inv), cvtpk(o1[0] * inv, o1[1] * inv), cvtpk(o1[2] * inv, o1[3] * inv)};
    if (kh == 1 && hf) { of[2] = 0u; of[3] = 0u; }
#pragma unroll
    for (int i = 0; i < 4; ++i) of[i] = min(of[i], SENT - 1u);
    __builtin_amdgcn_raw_buffer_store_b128(of, rws, WS_OB + (qb * 4 + hg) * 8192 + hh * 2048 + kh * 1024 + lane * 16, 0, SC1);
    st128(lds + L_OS + hg * 8192 + hh * 2048 + kh * 1024 + lane * 16, of);
    if (w < 7) st128(lds + L_YT + tid * 16, yreg);
  }
#pragma unroll
  for (int i = 0; i < 4; ++i) if (w < (i < 3 ? 7 : 4)) dma(rwo, wovoff, (unsigned)((112 * glate + 32 * i) * 896), ldsb + L_WO3 + (7 * i + w) * 1024);
  u32x4 in0, in1, in2;
  const int ib = WS_OB + qb * 32768 + tid * 16;
  auto fetch = [&]() {
    MEMFENCE();
    in0 = __builtin_amdgcn_raw_buffer_load_b128(rws, ib, ((hg + 1) & 3) * 8192, SC1);
    in1 = __builtin_amdgcn_raw_buffer_load_b128(rws, ib, ((hg + 2) & 3) * 8192, SC1);
    in2 = __builtin_amdgcn_raw_buffer_load_b128(rws, ib, ((hg + 3) & 3) * 8192, SC1);
    MEMFENCE(); };
  fetch();
  STAMP(11);
  BAR_LDS();
  f32x4 acc0 = {}, acc1 = {};
  const int arow = 16 * (g4 >> 1) + 4 * (g4 & 1) + tq;
  const int aoff0 = arow * 224 + 32 * w6 + 8 * tp, aoff1 = aoff0 + (g4 == 3 ? 0 : 8 * 224);
  const lptr ob = lds + L_OS + g4 * 512 + c15 * 16;
  struct OF { u32x4 a[4], b0[4], b1[4]; };
  auto oread = [&](int gq) { OF f;
    const lptr wb = lds + (gq == glate ? L_WO3 : L_WO + (gq - g0) * 25088);
#pragma unroll
    for (int h = 0; h < 4; ++h) {
      f.a[h] = cat(trd(wb + aoff0 + h * 6272), trd(wb + aoff1 + h * 6272));
      f.b0[h] = ld128(ob + gq * 8192 + h * 2048); f.b1[h] = ld128(ob + gq * 8192 + h * 2048 + 256);
    }
    SBAR(); return f; };
  auto omul = [&](const OF& f) {
#pragma unroll
    for (int h = 0; h < 4; ++h) { acc0 = MFMA16(asfrag(f.a[h]), asfrag(f.b0[h]), acc0); acc1 = MFMA16(asfrag(f.a[h]), asfrag(f.b1[h]), acc1); }
    SBAR(); };
  float pre0[4], pre1[4];
  if (w < 7) {
    omul(oread(hg));
    const lptr yp = lds + L_YT + (16 * w + 4 * g4 + tq) * 64 + 8 * tp;
    const u32x2 y0 = trd(yp), y1 = trd(yp + 32);
    const float om0 = rbf(1.f - __uint_as_float(gt0)), om1 = rbf(1.f - __uint_as_float(gt1));
#pragma unroll
    for (int i = 0; i < 4; ++i) {
      const float dsk = (i & 1) ? bfhi(dk[i >> 1]) : bflo(dk[i >> 1]);
      const float xa = (i & 1) ? bfhi(xe0[i >> 1]) : bflo(xe0[i >> 1]), xb = (i & 1) ? bfhi(xe1[i >> 1]) : bflo(xe1[i >> 1]);
      const float ya = (i & 1) ? bfhi(y0[i >> 1]) : bflo(y0[i >> 1]), yb = (i & 1) ? bfhi(y1[i >> 1]) : bflo(y1[i >> 1]);
      pre0[i] = rbf(om0 * rbf(ya + rbf(xa * dsk)));
      pre1[i] = rbf(om1 * rbf(yb + rbf(xb * dsk)));
    }
  } else if (b == 0) {
#pragma unroll
    for (int i = 0; i < 4; ++i) waitflags(P.ws, WS_F0 + (4 * lane + i) * 64, tag);
    if (lane == 0) stflag(P.ws, WS_E, epoch + 1);
  }
  for (unsigned spin = 0;; ++spin) {
    unsigned mx = 0;
#pragma unroll
    for (int i = 0; i < 4; ++i) mx = max(mx, max(in0[i], max(in1[i], in2[i])));
    if (__all(mx != SENT) || spin > SPIN_CAP) break;
    fetch();
  }
  st128(lds + L_OS + ((hg + 1) & 3) * 8192 + tid * 16, in0);
  st128(lds + L_OS + ((hg + 2) & 3) * 8192 + tid * 16, in1);
  st128(lds + L_OS + ((hg + 3) & 3) * 8192 + tid * 16, in2);
  STAMP(12);
  BAR_ALL();
  STAMP(13);
  if (w < 7) {
    const OF fa = oread((hg + 1) & 3), fb = oread((hg + 2) & 3);
    omul(fa);
    const OF fc = oread((hg + 3) & 3);
    omul(fb); omul(fc);
    const float ga = __uint_as_float(gt0), gb = __uint_as_float(gt1);
    float ra[4], rc[4];
#pragma unroll
    for (int i = 0; i < 4; ++i) { ra[i] = rbf(ga * rbf(acc0[i])) + pre0[i]; rc[i] = rbf(gb * rbf(acc1[i])) + pre1[i]; }
    u16* op = P.out + (32 * qb + c15) * D + n0 + 4 * g4;
    *(GAS u32x2*)op = (u32x2){cvtpk(ra[0], ra[1]), cvtpk(ra[2], ra[3])};
    *(GAS u32x2*)(op + 16 * D) = (u32x2){cvtpk(rc[0], rc[1]), cvtpk(rc[2], rc[3])};
  }
}

extern "C" void kernel_launch(void* const* d_in, const int* in_sizes, int n_in, void* d_out, int out_size, void* d_ws, size_t ws_size,
                              hipStream_t stream) {
  Params p;
  p.x = (const u16*)d_in[0]; p.wq = (const u16*)d_in[1]; p.wk = (const u16*)d_in[2]; p.wv = (const u16*)d_in[3]; p.wo = (const u16*)d_in[4];
  p.are = (const u16*)d_in[6]; p.aim = (const u16*)d_in[7]; p.bre = (const u16*)d_in[8]; p.bim = (const u16*)d_in[9];
  p.cre = (const u16*)d_in[10]; p.cim = (const u16*)d_in[11]; p.logdt = (const u16*)d_in[12]; p.dsk = (const u16*)d_in[13]; p.gw = (const u16*)d_in[14];
  p.out = (u16*)d_out; p.ws = (char*)d_ws;
  gated_mix_kernel<<<256, 512, 0, stream>>>(p);
}
```

```cpp
#include <hip/hip_runtime.h>
#include <hip/hip_bf16.h>
#include <stdint.h>

typedef unsigned short u16;
typedef unsigned long long u64;
using f32x2 = __attribute__((ext_vector_type(2))) float;
using f32x4 = __attribute__((ext_vector_type(4))) float;
using f32x16 = __attribute__((ext_vector_type(16))) float;
using bf16x2 = __attribute__((ext_vector_type(2))) __bf16;
using bf16x8 = __attribute__((ext_vector_type(8))) __bf16;
using s16x4 = __attribute__((ext_vector_type(4))) short;
using u32x2 = __attribute__((ext_vector_type(2))) unsigned;
using u32x4 = __attribute__((ext_vector_type(4))) unsigned;
typedef __amdgpu_buffer_rsrc_t rsrc_t;
#define LAS __attribute__((address_space(3)))
#define GAS __attribute__((address_space(1)))
#define DI __device__ __forceinline__
typedef LAS char* lptr;

constexpr int S = 2048, D = 448;
constexpr int WS_E = 0, WS_F3 = 2048, WS_F0 = 4096, WS_F1 = WS_F0 + 16384, WS_F2 = WS_F1 + 16384, WS_G = WS_F2 + 16384;
constexpr int F4 = 8;
constexpr int WS_KF = 65536, WS_VF = WS_KF + 131072, WS_Q = WS_VF + 131072, WS_XT = WS_Q + S * D * 2, WS_YT = WS_XT + S * D * 2;
constexpr int WS_OB = WS_YT + S * D * 2, WS_WF = WS_OB + 2097152, WS_END = WS_WF + 458752;
constexpr int L_SCR = 0, SCR_W = 4352, TROW = 272, L_X = 17408, L_W = 74752, L_WV = L_W + 25600, L_SL = 132096, L_WOI = L_SCR + 1024;
constexpr int L_AC = 86016, AC_COPY = 4160, AC_CH = 4 * AC_COPY, L_U = 149504, U_G = 1280, U_CH = 4 * U_G, L_TS = 159744, L_MISC = 162816;
constexpr int L_K0 = 0, L_QS = 4096, L_A = 20480, L_B = 86016, L_C = 0, L_LS = L_C + 16384, L_OS = L_A, L_YT = L_A + 32768;
constexpr int LDS_BYTES = 163840;
constexpr int M_KMAX = L_MISC, M_EPOCH = L_MISC + 8, M_CNT = L_MISC + 16;
constexpr unsigned SENT = 0xFFFFFFFFu;
constexpr int SC1 = 16;
constexpr unsigned SPIN_CAP = 1u << 15;
#define OUT_AUX 0
#define MID_KS0 2
#define MID_KS1 15
#define MID_KS2 22
#define NPRE 12
#define PROJ_PD 6
#define ATT_VAR 2
#define TAIL_VAR 1
#define CODE_PF_KB 40

struct Params {
  const u16 *x, *wq, *wk, *wv, *wo, *are, *aim, *bre, *bim, *cre, *cim, *logdt, *dsk, *gw;
  u16* out;
  char* ws;
};

#define MFMA32(a, b, c) __builtin_amdgcn_mfma_f32_32x32x16_bf16((a), (b), (c), 0, 0, 0)
#define MFMA16(a, b, c) __builtin_amdgcn_mfma_f32_16x16x32_bf16((a), (b), (c), 0, 0, 0)
#define SBAR() __builtin_amdgcn_sched_barrier(0)
#define PIN(x) asm volatile("" : "+v"(x))
#define MEMFENCE() asm volatile("" ::: "memory")
#define BAR_LDS() asm volatile("s_waitcnt lgkmcnt(0)\n\ts_barrier" ::: "memory")
#define BAR_ALL() asm volatile("s_waitcnt vmcnt(0) lgkmcnt(0)\n\ts_barrier" ::: "memory")
#define LDQ 16
#define LDSLEEP 3
#define PRE_W 2
#define F1_POS 0
#define P1_PRIO 0
#define XT_POS 2
#define THROTTLE() asm volatile("s_waitcnt vmcnt(%0)" :: "i"(LDQ - 1) : "memory")
#define DRAIN() asm volatile("s_waitcnt vmcnt(0)" ::: "memory")

#define STAMP(i) do {} while (0)
#define KSTAMP(g, i) do {} while (0)

DI float bf2f(unsigned b) { return __uint_as_float(b << 16); }
DI float bflo(unsigned w) { return __uint_as_float(w << 16); }
DI float bfhi(unsigned w) { return __uint_as_float(w & 0xFFFF0000u); }
DI unsigned cvtpk(float a, float b) { f32x2 v = {a, b}; return __builtin_bit_cast(unsigned, __builtin_convertvector(v, bf16x2)); }
DI float rbf(float a) { return bflo(cvtpk(a, a) & 0xFFFFu); }
DI bf16x8 asfrag(u32x4 v) { return __builtin_bit_cast(bf16x8, v); }
DI u32x4 ld128(lptr p) { return *(const LAS u32x4*)p; }
DI u32x2 ld64(lptr p) { return *(const LAS u32x2*)p; }
DI void st128(lptr p, u32x4 v) { *(LAS u32x4*)p = v; }
DI u32x2 trd(lptr p) { return __builtin_bit_cast(u32x2, __builtin_amdgcn_ds_read_tr16_b64_v4i16((LAS s16x4*)p)); }
DI u32x4 cat(u32x2 a, u32x2 b) { return (u32x4){a[0], a[1], b[0], b[1]}; }
DI rsrc_t mkrs(const void* p, int bytes) { return __builtin_amdgcn_make_buffer_rsrc((void*)p, (short)0, bytes, 0x00020000); }
DI float dot2(unsigned a, unsigned b, float c) { return __builtin_amdgcn_fdot2_f32_bf16(__builtin_bit_cast(bf16x2, a), __builtin_bit_cast(bf16x2, b), c, false); }
DI float swap32sum(float v) { auto r = __builtin_amdgcn_permlane32_swap(__float_as_uint(v), __float_as_uint(v), false, false); return __uint_as_float(r[0]) + __uint_as_float(r[1]); }
DI float swap32max(float v) { auto r = __builtin_amdgcn_permlane32_swap(__float_as_uint(v), __float_as_uint(v), false, false); return fmaxf(__uint_as_float(r[0]), __uint_as_float(r[1])); }
DI void dma(rsrc_t rs, unsigned voff, unsigned soff, unsigned dst) {
  asm volatile("s_mov_b32 m0, %0\n\ts_nop 0\n\tbuffer_load_dwordx4 %1, %2, %3 offen lds" :: "s"(dst), "v"(voff), "s"(rs), "s"(soff) : "memory");
}
DI void dma_sc1(rsrc_t rs, unsigned voff, unsigned soff, unsigned dst) {
  asm volatile("s_mov_b32 m0, %0\n\ts_nop 0\n\tbuffer_load_dwordx4 %1, %2, %3 offen sc1 lds" :: "s"(dst), "v"(voff), "s"(rs), "s"(soff) : "memory");
}
DI unsigned ldu32(const void* base, unsigned voff) { unsigned r; asm volatile("global_load_dword %0, %1, %2" : "=v"(r) : "v"(voff), "s"(base) : "memory"); return r; }
DI unsigned ldu16(const void* base, unsigned voff) { unsigned r; asm volatile("global_load_ushort %0, %1, %2" : "=v"(r) : "v"(voff), "s"(base) : "memory"); return r; }
template <int CTRL> DI unsigned dppmax(unsigned v) { return max(v, (unsigned)__builtin_amdgcn_update_dpp(0, (int)v, CTRL, 0xF, 0xF, true)); }
DI unsigned rowmax16(unsigned v) { v = dppmax<0xB1>(v); v = dppmax<0x4E>(v); v = dppmax<0x141>(v); return dppmax<0x140>(v); }
DI f32x2 cmul(f32x2 x, f32x2 z) {
  f32x2 t = x * (f32x2){z[0], z[0]};
  return __builtin_elementwise_fma((f32x2){x[1], x[0]}, (f32x2){-z[1], z[1]}, t);
}
DI u64 ldflag(const char* ws, int off) { return __hip_atomic_load((const GAS u64*)(ws + off), __ATOMIC_RELAXED, __HIP_MEMORY_SCOPE_AGENT); }
DI void stflag(char* ws, int off, u64 v) { __hip_atomic_store((GAS u64*)(ws + off), v, __ATOMIC_RELAXED, __HIP_MEMORY_SCOPE_AGENT); }
DI u64 waitflags(const char* ws, int off, u64 tag) {
  u64 v;
  for (unsigned spin = 0;; ++spin) {
    MEMFENCE();
    v = ldflag(ws, off);
    if (__all((v >> 16) == tag) || spin > SPIN_CAP) break;
    __builtin_amdgcn_s_sleep(1);
  }
  MEMFENCE();
  return v;
}

template <int WSTEP, int WHALF, bool SWAP, class F>
DI f32x16 proj_loop(lptr wa, lptr xb0, lptr xb1, lptr xb2, lptr xb3, F mid) {
  constexpr int PD = PROJ_PD;
  f32x16 acc = {};
  u32x4 wf[PD], xf[PD];
  auto rd = [&](int ks) {
    wf[ks % PD] = cat(trd(wa + ks * WSTEP), trd(wa + ks * WSTEP + WHALF));
    xf[ks % PD] = ld128(((ks & 3) == 0 ? xb0 : (ks & 3) == 1 ? xb1 : (ks & 3) == 2 ? xb2 : xb3) + (ks >> 2) * 128); };
#pragma unroll
  for (int ks = 0; ks < PD; ++ks) rd(ks);
  SBAR();
#pragma unroll
  for (int ks = 0; ks < 28; ++ks) {
    acc = SWAP ? MFMA32(asfrag(xf[ks % PD]), asfrag(wf[ks % PD]), acc) : MFMA32(asfrag(wf[ks % PD]), asfrag(xf[ks % PD]), acc);
    SBAR();
    if (ks + PD < 28) rd(ks + PD);
    SBAR();
    mid(ks); SBAR();
  }
  return acc;
}

struct Tab { unsigned v[4][16]; };
DI Tab build_tables(float dt, float are, float aim, float bre, float bim, float cr, float ci) {
  Tab pk;
  const float Lr = dt * are * 1.4426950408889634f, Rv = dt * aim * 0.15915494309189535f, br = dt * bre, bi = dt * bim;
  const f32x2 W = {cr * br - ci * bi, cr * bi + ci * br};
  auto zexp = [&](float g) { const float e = __builtin_amdgcn_exp2f(Lr * g), r = __builtin_amdgcn_fractf(Rv * g); return (f32x2){e * __builtin_amdgcn_cosf(r), e * __builtin_amdgcn_sinf(r)}; };
  auto put = [&](int t, int row, f32x2 v) { pk.v[t][row] = cvtpk(v[0], v[1]); };
  f32x2 e[16], Z[9], pw[8], wz[8];
  e[1] = zexp(8.f); e[1][1] = -e[1][1];
  const f32x2 z1 = zexp(1.f);
  Z[1] = zexp(128.f);
  e[2] = cmul(e[1], e[1]); e[3] = cmul(e[2], e[1]); e[4] = cmul(e[2], e[2]);
#pragma unroll
  for (int i = 1; i < 5; ++i) e[4 + i] = cmul(e[i], e[4]);
#pragma unroll
  for (int i = 1; i < 8; ++i) e[8 + i] = cmul(e[i], e[8]);
  pk.v[3][0] = 0x00003F80u;
#pragma unroll
  for (int i = 1; i < 16; ++i) { put(3, i, e[i]); PIN(pk.v[3][i]); }
  const f32x2 z2 = cmul(z1, z1);
  pw[0] = W; pw[1] = cmul(W, z1); Z[2] = cmul(Z[1], Z[1]);
  const f32x2 z4 = cmul(z2, z2);
  pw[2] = cmul(pw[0], z2); pw[3] = cmul(pw[1], z2); Z[3] = cmul(Z[2], Z[1]); Z[4] = cmul(Z[2], Z[2]);
#pragma unroll
  for (int i = 0; i < 4; ++i) pw[4 + i] = cmul(pw[i], z4);
  wz[0] = W; wz[1] = cmul(W, Z[1]);
  put(0, 8, wz[1]);
#pragma unroll
  for (int i = 0; i < 8; ++i) { put(0, i, pw[i]); if (i) put(0, 8 + i, cmul(pw[i], Z[1])); }
#pragma unroll
  for (int i = 0; i < 16; ++i) PIN(pk.v[0][i]);
#pragma unroll
  for (int i = 1; i < 5; ++i) Z[4 + i] = cmul(Z[i], Z[4]);
#pragma unroll
  for (int k = 2; k < 8; ++k) wz[k] = cmul(W, Z[k]);
#pragma unroll
  for (int k = 0; k < 2; ++k) { put(1, 4 * k, wz[2 + k]);
#pragma unroll
    for (int i = 1; i < 4; ++i) put(1, 4 * k + i, cmul(pw[2 * i], Z[2 + k])); }
#pragma unroll
  for (int i = 0; i < 4; ++i) { put(1, 8 + 2 * i, wz[4 + i]); put(1, 9 + 2 * i, cmul(pw[4], Z[4 + i])); }
  const f32x2 w8 = cmul(W, Z[8]);
  put(2, 0, w8); put(2, 8, cmul(w8, Z[8]));
#pragma unroll
  for (int i = 1; i < 8; ++i) put(2, i, cmul(wz[i], Z[8]));
#pragma unroll
  for (int i = 0; i < 16; ++i) { PIN(pk.v[1][i]); if (i < 9) PIN(pk.v[2][i]); }
  return pk;
}
struct Frg { u32x4 v[4][4]; };
#define WT(r) "ds_write_addtid_b32 %" #r " offset:(" #r "-1)*272\n\t"
DI Frg stage_tables(const Tab& pk, unsigned scb, lptr tr) {
  Frg f;
#pragma unroll
  for (int tt = 0; tt < 4; ++tt) {
    const int t = (tt + 3) & 3;
    asm volatile("s_mov_b32 m0, %0\n\ts_nop 0\n\t" WT(1) WT(2) WT(3) WT(4) WT(5) WT(6) WT(7) WT(8) WT(9)
                 :: "s"(scb), "v"(pk.v[t][0]), "v"(pk.v[t][1]), "v"(pk.v[t][2]), "v"(pk.v[t][3]), "v"(pk.v[t][4]), "v"(pk.v[t][5]), "v"(pk.v[t][6]), "v"(pk.v[t][7]), "v"(pk.v[t][8]) : "memory");
    if (t != 2) asm volatile("s_mov_b32 m0, %0\n\ts_nop 0\n\t" WT(1) WT(2) WT(3) WT(4) WT(5) WT(6) WT(7)
                 :: "s"(scb + 9 * TROW), "v"(pk.v[t][9]), "v"(pk.v[t][10]), "v"(pk.v[t][11]), "v"(pk.v[t][12]), "v"(pk.v[t][13]), "v"(pk.v[t][14]), "v"(pk.v[t][15]) : "memory");
#pragma unroll
    for (int ks = 0; ks < 4; ++ks) f.v[t][ks] = ld128(tr + ks * 64);
    MEMFENCE(); SBAR();
  }
  return f;
}

DI f32x16 attn_safe(rsrc_t rws, int lane, u32x4 q0, u32x4 q1) {
  if (lane >= 32) q1[2] = 0u;
  f32x16 o = {};
  float m = -INFINITY;
#pragma unroll 1
  for (int kb = 0; kb < 64; ++kb) {
    const u32x4 k0 = __builtin_amdgcn_raw_buffer_load_b128(rws, WS_KF + lane * 16, kb * 2048, SC1);
    const u32x4 k1 = __builtin_amdgcn_raw_buffer_load_b128(rws, WS_KF + 1024 + lane * 16, kb * 2048, SC1);
    const u32x4 v0 = __builtin_amdgcn_raw_buffer_load_b128(rws, WS_VF + lane * 16, kb * 2048, SC1);
    const u32x4 v1 = __builtin_amdgcn_raw_buffer_load_b128(rws, WS_VF + 1024 + lane * 16, kb * 2048, SC1);
    f32x16 s = {};
    s = MFMA32(asfrag(k0), asfrag(q0), s);
    s = MFMA32(asfrag(k1), asfrag(q1), s);
    float t = s[0];
#pragma unroll
    for (int i = 1; i < 16; ++i) t = fmaxf(t, s[i]);
    const float mn = fmaxf(m, swap32max(t));
    const float al = __builtin_amdgcn_exp2f(m - mn);
    m = mn;
    u32x4 p0, p1;
#pragma unroll
    for (int i = 0; i < 4; ++i) {
      p0[i] = cvtpk(__builtin_amdgcn_exp2f(s[2 * i] - mn), __builtin_amdgcn_exp2f(s[2 * i + 1] - mn));
      p1[i] = cvtpk(__builtin_amdgcn_exp2f(s[8 + 2 * i] - mn), __builtin_amdgcn_exp2f(s[9 + 2 * i] - mn));
    }
#pragma unroll
    for (int i = 0; i < 16; ++i) o[i] *= al;
    o = MFMA32(asfrag(v0), asfrag(p0), o);
    o = MFMA32(asfrag(v1), asfrag(p1), o);
  }
  return o;
}

struct Att {
  f32x16 sa, sb, o;
  u32x4 q0, q1, k0, k1, v0, v1, vp1, p0, p1;
};
#define EX(v) __builtin_amdgcn_exp2f(v)
template <int I, class F1, class F2>
DI void att_step(Att& a, f32x16& SC, f32x16& SN, lptr rdA, lptr rdB, F1 f1, F2 f2) {
  constexpr bool MORE = I < 31;
  constexpr int NS = I + 1;
  const lptr rd = ((NS >> 3) & 1) ? rdB + (NS & 7) * 8192 : rdA + (NS & 7) * 8192;
  u32x4 nk0, nk1, nv0, nv1;
  float e0, e1, e2, e3;
  const f32x16 zero = {};
  if constexpr (MORE) SN = MFMA32(asfrag(a.k0), asfrag(a.q0), zero);
  SBAR();
  if constexpr (MORE) { if constexpr (I < 30) nk0 = ld128(rd); nv0 = ld128(rd + 2048); }
  e0 = EX(SC[0]); e1 = EX(SC[1]); e2 = EX(SC[2]); e3 = EX(SC[3]);
  a.p0[0] = cvtpk(e0, e1); a.p0[1] = cvtpk(e2, e3);
  PIN(a.p0); SBAR();
  if constexpr (I > 0) a.o = MFMA32(asfrag(a.vp1), asfrag(a.p1), a.o);
  SBAR();
  f1();
  e0 = EX(SC[4]); e1 = EX(SC[5]); e2 = EX(SC[6]); e3 = EX(SC[7]);
  a.p0[2] = cvtpk(e0, e1); a.p0[3] = cvtpk(e2, e3);
  PIN(a.p0); SBAR();
  if constexpr (MORE) SN = MFMA32(asfrag(a.k1), asfrag(a.q1), SN);
  SBAR();
  if constexpr (MORE) { if constexpr (I < 30) nk1 = ld128(rd + 1024); nv1 = ld128(rd + 3072); }
  e0 = EX(SC[8]); e1 = EX(SC[9]); e2 = EX(SC[10]); e3 = EX(SC[11]);
  a.p1[0] = cvtpk(e0, e1); a.p1[1] = cvtpk(e2, e3);
  PIN(a.p1); SBAR();
  a.o = MFMA32(asfrag(a.v0), asfrag(a.p0), a.o);
  SBAR();
  f2();
  e0 = EX(SC[12]); e1 = EX(SC[13]); e2 = EX(SC[14]); e3 = EX(SC[15]);
  a.p1[2] = cvtpk(e0, e1); a.p1[3] = cvtpk(e2, e3);
  PIN(a.p1); SBAR();
  a.vp1 = a.v1;
  if constexpr (MORE) { a.v0 = nv0; a.v1 = nv1; if constexpr (I < 30) { a.k0 = nk0; a.k1 = nk1; } }
}

__global__ void __launch_bounds__(512, 2) gated_mix_kernel(Params P) {
  __shared__ __attribute__((aligned(16))) char lds_raw[LDS_BYTES];
  const lptr lds = (lptr)lds_raw;
  const unsigned ldsb = (unsigned)(uintptr_t)lds;
  u64 pc0; asm volatile("s_getpc_b64 %0" : "=s"(pc0));
  STAMP(0);
  const int tid = threadIdx.x, lane = tid & 63, w = __builtin_amdgcn_readfirstlane(tid >> 6);
  const int b = blockIdx.x, xcd = b & 7, j = b >> 3;
  const int rb = 4 * xcd + (j >> 3), ct = j & 7;
  const int qb = 8 * xcd + (j >> 2), hg = j & 3;
  const bool isS = ct < 7;
  const int cp = 7 * rb + ct;
  const int r31 = lane & 31, hf = lane >> 5, c15 = lane & 15, g4 = lane >> 4, tq = c15 >> 2, tp = lane & 3;
  const rsrc_t rws = mkrs(P.ws, WS_END), rx = mkrs(P.x, S * D * 2);

  if (w < 2 && j + 32 * w < CODE_PF_KB) {
    const rsrc_t rc = mkrs((const void*)(pc0 & ~(u64)255), CODE_PF_KB * 1024);
    dma(rc, (unsigned)(lane * 16), (unsigned)((j + 32 * w) * 1024), ldsb + L_TS);
  }
  if (w >= 4) __builtin_amdgcn_s_setprio(1);
  asm volatile("" :: "s"(P.x), "s"(P.ws));
  KSTAMP(1, 0);
  u64 epoch_v;
  auto ld_epoch = [&]() { asm volatile("global_load_dwordx2 %0, %1, %2 sc1" : "=v"(epoch_v) : "v"(0u), "s"(P.ws) : "memory"); };
  u64 epoch, tag, f0;
  auto mktag = [&]() {
    PIN(epoch_v);
    epoch = ((u64)__builtin_amdgcn_readfirstlane((unsigned)(epoch_v >> 32)) << 32) | (unsigned)__builtin_amdgcn_readfirstlane((unsigned)epoch_v);
    u64 z = epoch + 0x9E3779B97F4A7C15ull; z = (z ^ (z >> 30)) * 0xBF58476D1CE4E5B9ull; z = (z ^ (z >> 27)) * 0x94D049BB133111EBull; tag = (z ^ (z >> 31)) >> 16; };
  auto ldswait = [&](int off, int n) {
    MEMFENCE();
    while (__builtin_amdgcn_readfirstlane(__hip_atomic_load((LAS unsigned*)(lds + off), __ATOMIC_RELAXED, __HIP_MEMORY_SCOPE_WORKGROUP)) < n) __builtin_amdgcn_s_sleep(0);
    MEMFENCE(); };
  const int f0o = WS_F0 + ((cp >> 5) * 32 + r31) * 64;
  lptr ga[11];
  const int ch2 = w >> 2, wq = w & 3, l0 = 2040 - 8 * (tid & 255);
  auto mkaddr = [&]() {
    auto look = [&](int l, int lseg) {
      const int e = (lseg >= 256) + (lseg >= 512) + (lseg >= 1024);
      const int bias = ((1 << e) >> 1) - (e ? 1 : 0), bm = e ? 1 : 0;
      const int lr = ((l + bias + ((l >> e) & bm)) >> e) << e;
      const int row = 8 * e + ((lr >> 7) << (3 - e)) + ((lr & 7) >> e);
      return lds + L_TS + ch2 * 1536 + (row >> 2) * 128 + (row & 3) * 2 + (lr & 120); };
    const int l1 = l0 ? l0 : 8;
#pragma unroll
    for (int k = 0; k < 8; ++k) { ga[k] = look(l0 + 7 - k, l0); PIN(ga[k]); }
#pragma unroll
    for (int k = 0; k < 3; ++k) { ga[8 + k] = look(l1 - 1 - k, l1 - 8); PIN(ga[8 + k]); } };
  if (isS && w >= 4) {
    const int kc = 2 * cp + (wq >> 1);
    const unsigned po = (unsigned)(kc * 256 + (wq & 1) * 128 + 2 * lane) * 2u;
    unsigned pl = ldu16(P.logdt, (unsigned)kc * 2u);
    unsigned pa = ldu32(P.are, po), pb = ldu32(P.aim, po), pc = ldu32(P.bre, po), pd = ldu32(P.bim, po), pe = ldu32(P.cre, po), pf = ldu32(P.cim, po);
    asm volatile("s_barrier" ::: "memory");
#pragma unroll
    for (int i = 0; i < 2; ++i)
      dma(rx, (unsigned)((lane >> 3) * 896 + (lane & 7) * 16), (unsigned)((64 * rb + 16 * wq + 8 * i) * 896 + 128 * ct), ldsb + L_SL + (2 * wq + i) * 1024);
    ld_epoch();
    mkaddr();
    KSTAMP(1, 1);
    asm volatile("s_waitcnt vmcnt(3)" : "+v"(pa), "+v"(pb), "+v"(pc), "+v"(pd), "+v"(pe), "+v"(pf), "+v"(pl) :: "memory");
    STAMP(1);
    const lptr sc = lds + L_SCR + wq * SCR_W, tr = sc + c15 * TROW + g4 * 16;
    const unsigned scb = ldsb + L_SCR + wq * SCR_W;
    const float dt = rbf(expf(bf2f(pl)));
    const Tab ta = build_tables(dt, bflo(pa), bflo(pb), bflo(pc), bflo(pd), bflo(pe), bflo(pf));
    SBAR();
    KSTAMP(1, 2);
    SBAR();
    KSTAMP(1, 3);
    const Tab tb = build_tables(dt, bfhi(pa), bfhi(pb), bfhi(pc), bfhi(pd), bfhi(pe), bfhi(pf));
    SBAR();
    KSTAMP(1, 4);
    DRAIN();
#pragma unroll
    for (int i = 0; i < 2; ++i) {
      const lptr xs = lds + L_SL + (2 * wq + i) * 1024 + tq * 128 + g4 * 32 + tp * 8;
      __builtin_amdgcn_raw_buffer_store_b128(cat(trd(xs), trd(xs + 512)), rws, WS_XT + ((8 * rb + 2 * wq + i) * D + 64 * ct + lane) * 16, 0, SC1);
    }
    MEMFENCE(); mktag(); SBAR();
    const Frg fa = stage_tables(ta, scb, tr);
    KSTAMP(1, 5);
    DRAIN();
    if (lane == 0) {
      *(LAS u64*)(lds + M_EPOCH) = epoch;
      if (__hip_atomic_fetch_add((LAS unsigned*)(lds + M_CNT + 4), 1u, __ATOMIC_RELAXED, __HIP_MEMORY_SCOPE_WORKGROUP) == 3u)
        stflag(P.ws, WS_F0 + (ct * 32 + rb) * 64, tag << 16);
    }
    MEMFENCE();
    if (!P1_PRIO) __builtin_amdgcn_s_setprio(0);
    STAMP(2); STAMP(3);
    f32x4 kt[3] = {};
#pragma unroll
    for (int ks = 0; ks < 4; ++ks)
#pragma unroll
      for (int t = 0; t < 3; ++t) kt[t] = MFMA16(asfrag(fa.v[t][ks]), asfrag(fa.v[3][ks]), kt[t]);
    SBAR();
    const Frg fb = stage_tables(tb, scb, tr);
    f0 = ldflag(P.ws, f0o); MEMFENCE();
    KSTAMP(1, 6);
#pragma unroll
    for (int ks = 0; ks < 4; ++ks)
#pragma unroll
      for (int t = 0; t < 3; ++t) kt[t] = MFMA16(asfrag(fb.v[t][ks]), asfrag(fb.v[3][ks]), kt[t]);
#pragma unroll
    for (int t = 0; t < 3; ++t) st128(sc + t * 1024 + lane * 16, __builtin_bit_cast(u32x4, kt[t]));
  } else {
    if (isS) {
      const rsrc_t rw = mkrs(P.wq, D * D * 2);
      const unsigned wvoff = (unsigned)((112 * w + (lane >> 3)) * 896 + 128 * ct + (((lane & 7) ^ ((g4 & 1) << 2)) << 4));
      auto dma_w = [&](int ii) { const unsigned t = (unsigned)(ii + 4 * (rb & 3)), i = t - 14u * ((t * 19u) >> 8); dma(rw, wvoff, i * 7168u, ldsb + L_W + w * 14336 + i * 1024); };
      if (tid == 0) st128(lds + M_CNT, (u32x4){0u, 0u, 0u, 0u});
      asm volatile("s_waitcnt lgkmcnt(0)\n\ts_barrier" ::: "memory");
      __builtin_amdgcn_s_setprio(2);
      dma_w(0); dma_w(1); dma_w(2); dma_w(3);
      if (lane < 56) {
#pragma unroll
        for (int ii = 0; ii < 16; ++ii) {
          const int r = 16 * w + ((ii + 2 * ct) & 15);
          if (ii + 4 >= LDQ) THROTTLE();
          dma(rx, (unsigned)((lane ^ ((r >> 1) & 7)) << 4), (unsigned)((64 * rb + r) * 896), ldsb + L_X + r * 896);
        }
      }
#pragma unroll
      for (int ii = 4; ii < 14; ++ii) { THROTTLE(); dma_w(ii); }
      KSTAMP(1, 1);
      mkaddr();
    } else {
      ld_epoch();
      const rsrc_t rwk = mkrs(P.wk, D * 28 * 2), rwv = mkrs(P.wv, D * 28 * 2), rgw = mkrs(P.gw, D * 2);
      if (w == 7) dma(rgw, (unsigned)(lane * 16), 0u, ldsb + L_SCR);
      if (tid == 0) st128(lds + M_CNT, (u32x4){0u, 0u, 0u, 0u});
#pragma unroll
      for (int ii = 0; ii < 4; ++ii) {
        const int p = 8 * ((ii + rb) & 3) + w;
        if (p < 25) { dma(rwk, (unsigned)(lane * 16), (unsigned)(p * 1024), ldsb + L_W + p * 1024); dma(rwv, (unsigned)(lane * 16), (unsigned)(p * 1024), ldsb + L_WV + p * 1024); }
        if (ii == 0 && lane < 56) {
#pragma unroll
          for (int i = 0; i < 8; ++i) { const int r = 8 * w + i; dma(rx, (unsigned)((lane ^ ((r >> 1) & 7)) << 4), (unsigned)((64 * rb + r) * 896), ldsb + L_X + r * 896); }
        }
      }
    }
    STAMP(2);
    if (isS) {
      DRAIN();
      if (lane == 0) __hip_atomic_fetch_add((LAS unsigned*)(lds + M_CNT + 8), 1u, __ATOMIC_RELAXED, __HIP_MEMORY_SCOPE_WORKGROUP);
      ldswait(M_CNT + 8, 4);
    } else {
      __builtin_amdgcn_s_setprio(0);
      BAR_ALL();
    }
    STAMP(3);
  }
  if (!isS) mktag();

  u32x4 u0, u1;
  bool uearly = false;
  auto ldu = [&]() { MEMFENCE();
    const int uo = WS_XT + (((tid & 255) >> 1) * D + 2 * cp + (tid & 1)) * 16;
    u0 = __builtin_amdgcn_raw_buffer_load_b128(rws, uo, 0, SC1);
    u1 = __builtin_amdgcn_raw_buffer_load_b128(rws, uo, 128 * D * 16, SC1); MEMFENCE(); };
  auto put_f1 = [&]() {
    DRAIN();
    if (lane == 0 && __hip_atomic_fetch_add((LAS unsigned*)(lds + M_CNT), 1u, __ATOMIC_RELAXED, __HIP_MEMORY_SCOPE_WORKGROUP) == 3u)
      stflag(P.ws, WS_F1 + (ct * 32 + rb) * 64, tag << 16);
    MEMFENCE(); };
  if (w < 4) {
    if (!isS && tid == 0) stflag(P.ws, WS_F0 + (ct * 32 + rb) * 64, tag << 16);
    const int tt = w >> 1, row = 32 * tt + r31, f = ((row >> 1) & 7) ^ hf;
    const lptr xr = lds + L_X + row * 896;
    const lptr xb0 = xr + (f << 4), xb1 = xr + ((f ^ 2) << 4), xb2 = xr + ((f ^ 4) << 4), xb3 = xr + ((f ^ 6) << 4);
    if (isS) {
      const int mt = w & 1;
      const lptr wa = lds + L_W + (8 * hf + tq) * 128 + (((4 * mt + 2 * (g4 & 1) + (tp >> 1)) ^ ((tq >> 1) << 2)) << 4) + (tp & 1) * 8;
      const f32x16 acc = proj_loop<2048, 512, false>(wa, xb0, xb1, xb2, xb3, [&](int) {});
      { float t0 = acc[0]; asm volatile("" :: "v"(t0)); } KSTAMP(3, 0);
      constexpr float QS = 0.18898223650461362f * 1.4426950408889634f;
#pragma unroll
      for (int i = 0; i < 4; i += 2) {
        const auto r0 = __builtin_amdgcn_permlane32_swap(cvtpk(acc[4 * i] * QS, acc[4 * i + 1] * QS), cvtpk(acc[4 * i + 4] * QS, acc[4 * i + 5] * QS), false, false);
        const auto r1 = __builtin_amdgcn_permlane32_swap(cvtpk(acc[4 * i + 2] * QS, acc[4 * i + 3] * QS), cvtpk(acc[4 * i + 6] * QS, acc[4 * i + 7] * QS), false, false);
        __builtin_amdgcn_raw_buffer_store_b128((u32x4){r0[0], r1[0], r0[1], r1[1]}, rws, WS_Q + ((8 * ct + 4 * mt + i + hf) * S + 64 * rb + row) * 16, 0, SC1);
      }
      KSTAMP(3, 1);
      ldswait(M_CNT + 4, 1);
      epoch_v = __builtin_bit_cast(u64, ld64(lds + M_EPOCH));
      mktag();
      if (F1_POS == 0) put_f1();
      KSTAMP(3, 2);
    } else {
      const lptr wa = lds + L_W + (8 * hf + tq) * 56 + 32 * (g4 & 1) + 8 * tp;
      u32x4 f0, f1;
      const int off = ((w & 1) ? WS_VF : WS_KF) + (2 * rb + tt) * 2048 + lane * 16;
      if (w & 1) {
        const f32x16 acc = proj_loop<896, 224, true>(wa + (L_WV - L_W), xb0, xb1, xb2, xb3, [&](int) {});
#pragma unroll
        for (int i = 0; i < 4; ++i) { f0[i] = cvtpk(acc[2 * i], acc[2 * i + 1]); f1[i] = cvtpk(acc[8 + 2 * i], acc[9 + 2 * i]); }
        if (r31 >= 28) { const unsigned c = r31 == 28 ? 0x3F803F80u : 0u; f0 = (u32x4){c, c, c, c}; f1 = f0; }
        __builtin_amdgcn_raw_buffer_store_b128(f0, rws, off, 0, SC1);
        __builtin_amdgcn_raw_buffer_store_b128(f1, rws, off + 1024, 0, SC1);
      } else {
        const f32x16 acc = proj_loop<896, 224, false>(wa, xb0, xb1, xb2, xb3, [&](int) {});
#pragma unroll
        for (int i = 0; i < 4; ++i) { f0[i] = cvtpk(acc[2 * i], acc[2 * i + 1]); f1[i] = cvtpk(acc[8 + 2 * i], acc[9 + 2 * i]); }
        if (hf) { f1[2] = 0x00003F80u; f1[3] = 0u; }
        __builtin_amdgcn_raw_buffer_store_b128(f0, rws, off, 0, SC1);
        __builtin_amdgcn_raw_buffer_store_b128(f1, rws, off + 1024, 0, SC1);
        float n2 = hf ? -1.f : 0.f;
#pragma unroll
        for (int i = 0; i < 4; ++i) { n2 = dot2(f0[i], f0[i], n2); n2 = dot2(f1[i], f1[i], n2); }
        const unsigned nm = rowmax16(__float_as_uint(fmaxf(swap32sum(n2), 0.f)));
        const unsigned nw = max((unsigned)__builtin_amdgcn_readlane((int)nm, 0), (unsigned)__builtin_amdgcn_readlane((int)nm, 16));
        if (lane == 0) *(LAS unsigned*)(lds + M_KMAX + tt * 4) = nw;
      }
      asm volatile("s_waitcnt vmcnt(0) lgkmcnt(0)" ::: "memory");
      if (lane == 0 && __hip_atomic_fetch_add((LAS unsigned*)(lds + M_CNT), 1u, __ATOMIC_RELAXED, __HIP_MEMORY_SCOPE_WORKGROUP) == 3u) {
        const u32x2 km = ld64(lds + M_KMAX);
        stflag(P.ws, WS_F1 + (ct * 32 + rb) * 64, (tag << 16) | (u64)((max(km[0], km[1]) >> 16) + 1u));
      }
    }
  } else if (isS) {
    const int t4 = tid - 256;
    if (t4 < 128 && c15 != 15) st128(lds + L_U + (t4 >> 4) * U_G + c15 * 16, (u32x4){0u, 0u, 0u, 0u});
    uearly = __all((f0 >> 16) == tag);
    if (uearly) ldu();

  } else {
    if (lane < 56) {
      const rsrc_t rwo = mkrs(P.wo, D * D * 2);
      const int hi = lane >= 28 ? 1 : 0;
#pragma unroll
      for (int i = 0; i < 4; ++i) if (i < 3 || wq < 2)
        dma(rwo, (unsigned)(hi * 896 + (lane - 28 * hi) * 16), (unsigned)((28 * (rb >> 1) + 8 * i + 2 * wq) * 896 + (rb & 1) * 448), ldsb + L_WOI + (8 * i + 2 * wq) * 448);
    }
    const int row = (tid - 256) >> 2, sub = tid & 3, f = (row >> 1) & 7;
    float s = 0.f;
#pragma unroll
    for (int i = 0; i < 14; ++i) {
      const int c = sub + 4 * i;
      const u32x4 xv = ld128(lds + L_X + row * 896 + (((c & ~7) | ((c & 7) ^ f)) << 4));
      const u32x4 gv = ld128(lds + L_SCR + c * 16);
#pragma unroll
      for (int k = 0; k < 4; ++k) s = dot2(xv[k], gv[k], s);
    }
    s += __builtin_bit_cast(float, __builtin_amdgcn_update_dpp(0, __builtin_bit_cast(int, s), 0xB1, 0xF, 0xF, true));
    s += __builtin_bit_cast(float, __builtin_amdgcn_update_dpp(0, __builtin_bit_cast(int, s), 0x4E, 0xF, 0xF, true));
    const float gs = rbf(s), gt = rbf(1.f / (1.f + expf(-gs)));
    if (sub == 0) __builtin_amdgcn_raw_buffer_store_b32(__float_as_uint(gt), rws, WS_G + (64 * rb + row) * 4, 0, SC1);
  }
  __builtin_amdgcn_s_setprio(0);
  if (!isS && w >= 4) DRAIN();
  STAMP(4);
  BAR_LDS();
  STAMP(5);

  if (isS) {
    if (w >= 4 && !uearly) { waitflags(P.ws, f0o, tag); ldu(); }
    const int ch = ch2, m = tid & 255;
    f32x4 pv[2][2];
#pragma unroll
    for (int rr = 0; rr < 2; ++rr) {
      const int t = rr == 0 ? (wq < 2 ? 2 : wq == 2 ? 1 : 0) : ((wq & 1) ? 1 : 2);
      const lptr sp = lds + L_SCR + ch * 2 * SCR_W + t * 1024 + lane * 16;
#pragma unroll
      for (int i = 0; i < 2; ++i) pv[rr][i] = __builtin_bit_cast(f32x4, ld128(sp + i * SCR_W));
    }
#pragma unroll
    for (int rr = 0; rr < 2; ++rr) {
      const f32x4 v = pv[rr][0] + pv[rr][1];
      const int t = rr == 0 ? (wq < 2 ? 2 : wq == 2 ? 1 : 0) : ((wq & 1) ? 1 : 2);
      *(LAS u32x2*)(lds + L_TS + ch * 1536 + t * 512 + lane * 8) = (u32x2){cvtpk(v[0], v[1]), cvtpk(v[2], v[3])};
    }
    MEMFENCE();
    KSTAMP(3, 3);
    unsigned d[6];
#pragma unroll
    for (int k = 0; k < 5; ++k) d[k] = (unsigned)*(const LAS u16*)ga[2 * k] | ((unsigned)*(const LAS u16*)ga[2 * k + 1] << 16);
    d[5] = *(const LAS u16*)ga[10];
    if (l0 == 0) { d[4] = 0u; d[5] = 0u; }
    PIN(d[5]); KSTAMP(3, 4);
    if (F1_POS == 1 && w < 4) put_f1();
    unsigned al[5];
#pragma unroll
    for (int k = 0; k < 5; ++k) al[k] = __builtin_amdgcn_alignbit(d[k + 1], d[k], 16);
    const lptr dst = lds + L_AC + ch * AC_CH + m * 16;
    st128(dst, (u32x4){d[0], d[1], d[2], d[3]});
    st128(dst + AC_COPY, (u32x4){al[0], al[1], al[2], al[3]});
    st128(dst + 2 * AC_COPY, (u32x4){d[1], d[2], d[3], d[4]});
    st128(dst + 3 * AC_COPY, (u32x4){al[1], al[2], al[3], al[4]});
    KSTAMP(3, 5);
    if (w >= 4) {
      const int mm = (tid & 255) >> 1;
      const lptr du = lds + L_U + (tid & 1) * U_CH + (mm & 3) * U_G + ((mm >> 2) + 15) * 16;
      st128(du, u0); st128(du + 512, u1);
    } else if (tid < 32) st128(lds + L_AC + (tid >> 2) * AC_COPY + 4096 + (tid & 3) * 16, (u32x4){0u, 0u, 0u, 0u});
  } else {
    if (w < 7) {
#pragma unroll
      for (int i = 0; i < 2; ++i) {
        const lptr wp = lds + L_WOI + (16 * (g4 >> 1) + 4 * (g4 & 1) + tq) * 448 + (2 * w + i) * 32 + 8 * tp;
        const u32x2 lo = trd(wp);
        u32x2 hi = trd(wp + (g4 == 3 ? 0 : 8 * 448));
        if (g4 == 3) hi = (u32x2){0u, 0u};
        __builtin_amdgcn_raw_buffer_store_b128(cat(lo, hi), rws, WS_WF + ((14 * (rb & 1) + 2 * w + i) * 16 + (rb >> 1)) * 1024 + lane * 16, 0, SC1);
      }
    }
    DRAIN();
  }
  const int hh = w & 3, kh = w >> 2;
  const unsigned kvoff = (unsigned)(((w & 2) ? WS_VF : WS_KF + 2048) + (w & 1) * 1024 + lane * 16);
  const int f1o = WS_F1 + (hf ? min(((112 * hg) >> 6) + tp, (112 * hg + 111) >> 6) * 32 + rb : 7 * 32 + r31) * 64;
  u64 fl;
  if (w >= 4) { fl = ldflag(P.ws, f1o); MEMFENCE(); }
  STAMP(6);
  BAR_LDS();
  STAMP(7);
  if (!isS && tid == 256) stflag(P.ws, WS_F3 + rb * 64, tag << 16);

  Att a;
  if (w < 4) {
    if (isS) {
      const int ch = w >> 1, mt = w & 1, rp = 15 - c15;
      const lptr ab = lds + L_AC + ch * AC_CH + (rp & 3) * AC_COPY + 2 * (16 * (1 - mt) + (rp & 12) + 8 * g4);
      lptr ab2 = ab + 8; PIN(ab2);
      const lptr ub = lds + L_U + ch * U_CH + g4 * U_G + c15 * 16;
      f32x4 y[4] = {};
      u32x4 af[2][4], bfr[2][4];
      auto rda = [&](int d0, int i) { const int ao = 64 * (63 - d0 - 16 * i); af[d0 & 1][i] = cat(ld64(ab + ao), ld64(ab2 + ao)); };
      auto rdb = [&](int d0, int i) { bfr[d0 & 1][i] = ld128(ub + (15 - d0) * 16 + 256 * i); };
#pragma unroll
      for (int i = 0; i < 4; ++i) { rda(0, i); rdb(0, i); }
      SBAR();
#pragma unroll
      for (int d0 = 0; d0 < 16; ++d0) {
        const int c = d0 & 1;
        const bool more = d0 < 15;
#define CM(i, nt) y[nt] = MFMA16(asfrag(af[c][i]), asfrag(bfr[c][nt - i]), y[nt]); SBAR();
        CM(0, 0) if (more) rda(d0 + 1, 0); SBAR();
        CM(0, 1) if (more) rdb(d0 + 1, 0); SBAR();
        CM(0, 2) if (more) rdb(d0 + 1, 1); SBAR();
        CM(0, 3) if (more) rdb(d0 + 1, 2); SBAR();
        CM(1, 1) if (more) rdb(d0 + 1, 3); SBAR();
        CM(1, 2) if (more) rda(d0 + 1, 1); SBAR();
        CM(1, 3) if (more) rda(d0 + 1, 2); SBAR();
        CM(2, 2) if (more) rda(d0 + 1, 3); SBAR();
        CM(2, 3) CM(3, 3)
#undef CM
      }
#pragma unroll
      for (int nt = 0; nt < 4; nt += 2) {
        const auto r0 = __builtin_amdgcn_permlane16_swap(cvtpk(y[nt][0], y[nt][1]), cvtpk(y[nt + 1][0], y[nt + 1][1]), false, false);
        const auto r1 = __builtin_amdgcn_permlane16_swap(cvtpk(y[nt][2], y[nt][3]), cvtpk(y[nt + 1][2], y[nt + 1][3]), false, false);
        __builtin_amdgcn_raw_buffer_store_b128((u32x4){r0[0], r1[0], r0[1], r1[1]}, rws,
                                               WS_YT + (2 * cp + ch) * 4096 + mt * 2048 + (16 * (nt + (g4 & 1)) + c15) * 32 + (g4 >> 1) * 16, 0, SC1);
      }
    }
    else if (rb == 0 && w == 0) {
#pragma unroll
      for (int i = 0; i < 4; ++i) waitflags(P.ws, WS_F0 + (4 * lane + i) * 64, tag);
      if (lane == 0) stflag(P.ws, WS_E, epoch + 1);
    }
    STAMP(8);
    BAR_LDS();
  } else {
    if (!__all((fl >> 16) == tag)) fl = waitflags(P.ws, f1o, tag);
    KSTAMP(3, 6);
    const int qd0 = 7 * (4 * hg + hh) + hf;
    const int qo = WS_Q + ((qd0 >> 1) * S + 32 * qb + r31) * 16 + (qd0 & 1) * 8;
    u32x2 qa, qc, qd, qe;
    asm volatile("buffer_load_dwordx2 %0, %4, %5, 0 offen sc1\n\tbuffer_load_dwordx2 %1, %4, %5, %6 offen sc1\n\t"
                 "buffer_load_dwordx2 %2, %4, %5, %7 offen sc1\n\tbuffer_load_dwordx2 %3, %4, %5, %8 offen sc1"
                 : "=&v"(qa), "=&v"(qc), "=&v"(qd), "=&v"(qe) : "v"(qo), "s"(rws), "s"(S * 16), "s"(2 * S * 16), "s"(3 * S * 16) : "memory");
    dma_sc1(rws, (unsigned)(WS_KF + lane * 16), (unsigned)((hh >> 1) * 65536 + (hh & 1) * 1024), ldsb + L_K0 + hh * 1024);
#pragma unroll
    for (int ii = 0; ii < 16; ++ii) {
      const int i = (ii + j) & 15;
      dma_sc1(rws, kvoff, (unsigned)((i & 1) * 65536 + (i >> 1) * 2048), ldsb + L_A + i * 4096 + hh * 1024);
    }
    u32x4 t0, t1;
    asm volatile("buffer_load_dwordx4 %0, %2, %3, %4 offen sc1\n\tbuffer_load_dwordx4 %1, %2, %3, %4 offen offset:1024 sc1"
                 : "=&v"(t0), "=&v"(t1) : "v"(WS_KF + lane * 16), "s"(rws), "s"(j * 8192 + hh * 2048) : "memory");
    const unsigned kr = rowmax16((unsigned)(fl & 0xFFFFu));
    const unsigned km = max((unsigned)__builtin_amdgcn_readlane((int)kr, 0), (unsigned)__builtin_amdgcn_readlane((int)kr, 16));
    asm volatile("s_waitcnt vmcnt(19)" : "+v"(qa), "+v"(qc), "+v"(qd), "+v"(qe) :: "memory");
    KSTAMP(3, 7);
    if (hf) qe = (u32x2){0u, 0u};
    a.q0 = cat(qa, qc); a.q1 = cat(qd, qe);
    float n2 = 0.f;
#pragma unroll
    for (int i = 0; i < 4; ++i) { n2 = dot2(a.q0[i], a.q0[i], n2); n2 = dot2(a.q1[i], a.q1[i], n2); }
    n2 = swap32sum(n2);
    const float mb = __builtin_amdgcn_sqrtf(n2 * bf2f(km));
    if (hf) a.q1[2] = cvtpk(-mb, 0.f);
    st128(lds + L_QS + hh * 2048 + lane * 16, a.q0);
    st128(lds + L_QS + hh * 2048 + 1024 + lane * 16, a.q1);
    asm volatile("s_waitcnt vmcnt(0)" : "+v"(t0), "+v"(t1) :: "memory");
    STAMP(8);
    BAR_ALL();
  }
  STAMP(9); KSTAMP(2, 6);

  if (w < 4) { a.q0 = ld128(lds + L_QS + hh * 2048 + lane * 16); a.q1 = ld128(lds + L_QS + hh * 2048 + 1024 + lane * 16); }
  const lptr rdA = lds + L_A + kh * 4096 + lane * 16, rdB = lds + L_B + kh * 4096 + lane * 16;
  {
    const u32x4 f0 = ld128(lds + L_K0 + kh * 2048 + lane * 16), f1 = ld128(lds + L_K0 + kh * 2048 + 1024 + lane * 16);
    a.k0 = ld128(rdA); a.k1 = ld128(rdA + 1024); a.v0 = ld128(rdA + 2048); a.v1 = ld128(rdA + 3072);
    const f32x16 zero = {};
    a.sa = MFMA32(asfrag(f0), asfrag(a.q0), zero);
    a.sa = MFMA32(asfrag(f1), asfrag(a.q1), a.sa);
    a.o = zero; a.vp1 = a.v1; a.p1 = (u32x4){0u, 0u, 0u, 0u}; a.p0 = a.p1;
  }
  const unsigned kvs = (unsigned)(kh * 65536);
  auto fill = [&](int stage, int i) { dma_sc1(rws, kvoff, kvs + (unsigned)((8 * stage + i) * 2048), ldsb + ((stage & 1) ? L_B : L_A) + i * 8192 + w * 1024); };
#define PRIO(x) if (w >= 4) __builtin_amdgcn_s_setprio(x); SBAR();
  const int w6 = min(w, 6), n0 = 112 * hg + 16 * w6;
  u32x4 wf[16];
  auto ldwf = [&](int i) { wf[i] = __builtin_amdgcn_raw_buffer_load_b128(rws, WS_WF + lane * 16, ((7 * hg + w6) * 16 + ((4 * hg + i) & 15)) * 1024, SC1); MEMFENCE(); };
  const int f3o = WS_F3 + (hf ? rb : 2 * c15 + (7 * hg + w6 >= 14 ? 1 : 0)) * 64,
            f2o = WS_F2 + (56 * hg + 8 * w6 + (lane & 7)) * 64;
  u64 f2, f3;
  u32x4 yreg; u32x2 xe0, xe1, dk; unsigned gt0, gt1;
  auto mixld = [&]() {
    yreg = __builtin_amdgcn_raw_buffer_load_b128(rws, WS_YT + (n0 + (lane >> 2)) * 4096 + (tp >> 1) * 2048 + qb * 32 + (tp & 1) * 16, 0, SC1);
    xe0 = __builtin_amdgcn_raw_buffer_load_b64(rx, ((32 * qb + c15) * D + n0 + 4 * g4) * 2, 0, 0);
    xe1 = __builtin_amdgcn_raw_buffer_load_b64(rx, ((32 * qb + 16 + c15) * D + n0 + 4 * g4) * 2, 0, 0);
    MEMFENCE(); };
  auto mixld2 = [&]() {
    dk = *(const GAS u32x2*)(P.dsk + n0 + 4 * g4);
    gt0 = __builtin_amdgcn_raw_buffer_load_b32(rws, WS_G + (32 * qb + c15) * 4, 0, SC1);
    gt1 = __builtin_amdgcn_raw_buffer_load_b32(rws, WS_G + (32 * qb + 16 + c15) * 4, 0, SC1);
    MEMFENCE(); };
#define NOP [&]() {}
#define KV(s, i) [&]() { if (!(ATT_VAR & 4)) fill(s, i); }
#define WF(i) [&]() { if (!(ATT_VAR & 16)) ldwf(i); else wf[i] = a.q0; }
#define STEP(I, F1, F2) att_step<I>(a, (I & 1) ? a.sb : a.sa, (I & 1) ? a.sa : a.sb, rdA, rdB, F1, F2);
  SBAR();
#define BAR_CNT(n) asm volatile("s_waitcnt vmcnt(" #n ") lgkmcnt(0)\n\ts_barrier" ::: "memory")
  u64 f4;
  const int f4o = WS_F0 + F4 + (((j & 4) | ((hg + 1 + (tp % 3)) & 3)) * 32 + rb) * 64;
  f3 = ldflag(P.ws, f3o); MEMFENCE(); SBAR();
  PRIO(1) STEP(0, KV(1, 0), KV(1, 1)) STEP(1, KV(1, 2), KV(1, 3)) STEP(2, KV(1, 4), KV(1, 5)) STEP(3, KV(1, 6), KV(1, 7))
  PRIO(0) STEP(4, ([&]() { __builtin_amdgcn_raw_buffer_store_b128((u32x4){SENT, SENT, SENT, SENT}, rws, WS_OB + (qb * 4 + hg) * 8192 + tid * 16, 0, SC1); MEMFENCE(); }), NOP)
  STEP(5, NOP, NOP) STEP(6, NOP, NOP)
  KSTAMP(2, 0);
  if (!(ATT_VAR & 8)) BAR_CNT(1); SBAR();
  KSTAMP(2, 1);
  if (!__all((f3 >> 16) == tag)) waitflags(P.ws, f3o, tag);
  MEMFENCE(); SBAR();
  PRIO(1) STEP(7, KV(2, 0), KV(2, 1)) STEP(8, KV(2, 2), KV(2, 3)) STEP(9, KV(2, 4), KV(2, 5)) STEP(10, KV(2, 6), KV(2, 7))
  STEP(11, ([&]() { if (isS && tid == 0) stflag(P.ws, WS_F2 + cp * 64, tag << 16); MEMFENCE(); }), WF(0)) PRIO(0) STEP(12, WF(1), WF(2)) STEP(13, WF(3), WF(4)) STEP(14, WF(5), NOP)
  KSTAMP(2, 2);
  if (!(ATT_VAR & 8)) BAR_CNT(6); SBAR();
  KSTAMP(2, 3);
  PRIO(1) STEP(15, KV(3, 0), KV(3, 1)) STEP(16, KV(3, 2), KV(3, 3)) STEP(17, KV(3, 4), KV(3, 5)) STEP(18, KV(3, 6), KV(3, 7))
  STEP(19, ([&]() { f2 = ldflag(P.ws, f2o); if (tid == 64) stflag(P.ws, WS_F0 + F4 + (ct * 32 + rb) * 64, tag << 16); MEMFENCE(); }), WF(6))
  PRIO(0) STEP(20, WF(7), WF(8)) STEP(21, WF(9), WF(10)) STEP(22, WF(11), NOP)
  KSTAMP(2, 4);
  if (!(ATT_VAR & 8)) BAR_CNT(6); SBAR();
  KSTAMP(2, 5);
  if (!__all((f2 >> 16) == tag)) waitflags(P.ws, f2o, tag);
  MEMFENCE(); SBAR();
  PRIO(1) STEP(23, mixld, mixld2) STEP(24, WF(12), WF(13)) STEP(25, WF(14), WF(15)) STEP(26, NOP, NOP) STEP(27, ([&]() { f4 = ldflag(P.ws, f4o); MEMFENCE(); }), NOP)
  PRIO(0) STEP(28, NOP, NOP) STEP(29, NOP, NOP) STEP(30, NOP, NOP) STEP(31, NOP, NOP)
  a.o = MFMA32(asfrag(a.vp1), asfrag(a.p1), a.o);
  STAMP(10);

  {
    const lptr xo = lds + L_C + w * 2048 + lane * 16;
    if (kh == 0) { st128(xo, __builtin_bit_cast(u32x4, (f32x4){a.o[8], a.o[9], a.o[10], a.o[11]})); st128(xo + 1024, __builtin_bit_cast(u32x4, (f32x4){a.o[12], a.o[13], a.o[14], a.o[15]})); }
    else { st128(xo, __builtin_bit_cast(u32x4, (f32x4){a.o[0], a.o[1], a.o[2], a.o[3]})); st128(xo + 1024, __builtin_bit_cast(u32x4, (f32x4){a.o[4], a.o[5], a.o[6], a.o[7]})); }
    if (hf) *(LAS float*)(lds + L_LS + w * 128 + r31 * 4) = a.o[12];
    if (w < 7) st128(lds + L_YT + tid * 16, yreg);
  }
  if (!__all((f4 >> 16) == tag)) waitflags(P.ws, f4o, tag);
  u32x4 in0, in1, in2;
  const int ib = WS_OB + qb * 32768 + tid * 16;
  auto fetch = [&]() {
    MEMFENCE();
    in0 = __builtin_amdgcn_raw_buffer_load_b128(rws, ib, ((hg + 1) & 3) * 8192, SC1);
    in1 = __builtin_amdgcn_raw_buffer_load_b128(rws, ib, ((hg + 2) & 3) * 8192, SC1);
    in2 = __builtin_amdgcn_raw_buffer_load_b128(rws, ib, ((hg + 3) & 3) * 8192, SC1);
    MEMFENCE(); };
  if (TAIL_VAR == 4) fetch();
  BAR_LDS();
  STAMP(11); KSTAMP(2, 7);
  if (TAIL_VAR == 3) fetch();
  {
    const lptr xi = lds + L_C + (w ^ 4) * 2048 + lane * 16;
    f32x4 o0 = __builtin_bit_cast(f32x4, ld128(xi)), o1 = __builtin_bit_cast(f32x4, ld128(xi + 1024));
    float l = *(const LAS float*)(lds + L_LS + hh * 128 + r31 * 4) + *(const LAS float*)(lds + L_LS + (hh + 4) * 128 + r31 * 4);
    if (kh == 0) { o0 += (f32x4){a.o[0], a.o[1], a.o[2], a.o[3]}; o1 += (f32x4){a.o[4], a.o[5], a.o[6], a.o[7]}; }
    else { o0 += (f32x4){a.o[8], a.o[9], a.o[10], a.o[11]}; o1 += (f32x4){a.o[12], a.o[13], a.o[14], a.o[15]}; }
    if (__builtin_expect(!__all(l >= 0x1p-80f && l <= 0x1p80f), 0)) {
      const f32x16 so = attn_safe(rws, lane, a.q0, a.q1);
      l = swap32sum(hf ? so[12] : 0.f);
      if (kh == 0) { o0 = (f32x4){so[0], so[1], so[2], so[3]}; o1 = (f32x4){so[4], so[5], so[6], so[7]}; }
      else { o0 = (f32x4){so[8], so[9], so[10], so[11]}; o1 = (f32x4){so[12], so[13], so[14], so[15]}; }
    }
    const float inv = __builtin_amdgcn_rcpf(l);
    u32x4 of = {cvtpk(o0[0] * inv, o0[1] * inv), cvtpk(o0[2] * inv, o0[3] * inv), cvtpk(o1[0] * inv, o1[1] * inv), cvtpk(o1[2] * inv, o1[3] * inv)};
    if (kh == 1 && hf) { of[2] = 0u; of[3] = 0u; }
#pragma unroll
    for (int i = 0; i < 4; ++i) of[i] = min(of[i], SENT - 1u);
    __builtin_amdgcn_raw_buffer_store_b128(of, rws, WS_OB + (qb * 4 + hg) * 8192 + hh * 2048 + kh * 1024 + lane * 16, 0, SC1);
    st128(lds + L_OS + hg * 8192 + hh * 2048 + kh * 1024 + lane * 16, of);
  }
  if (TAIL_VAR == 0) fetch();
  STAMP(12);
  KSTAMP(4, 0);
  BAR_LDS();
  KSTAMP(4, 1);
  if (TAIL_VAR == 2) fetch();
  f32x4 acc0 = {}, acc1 = {};
  const lptr ob = lds + L_OS + g4 * 512 + c15 * 16;
  struct F4 { u32x4 v[4]; };
  auto bread = [&](int k, int rt) { F4 f;
#pragma unroll
    for (int h = 0; h < 4; ++h) f.v[h] = ld128(ob + ((hg + k) & 3) * 8192 + h * 2048 + rt * 256);
    return f; };
  auto omul = [&](int k, const F4& b0, const F4& b1) {
#pragma unroll
    for (int h = 0; h < 4; ++h) { acc0 = MFMA16(asfrag(wf[4 * k + h]), asfrag(b0.v[h]), acc0); acc1 = MFMA16(asfrag(wf[4 * k + h]), asfrag(b1.v[h]), acc1); } };
  float pre0[4], pre1[4];
  if (w < 7) {
    omul(0, bread(0, 0), bread(0, 1));
    const lptr yp = lds + L_YT + (16 * w + 4 * g4 + tq) * 64 + 8 * tp;
    const u32x2 y0 = trd(yp), y1 = trd(yp + 32);
    const float om0 = rbf(1.f - __uint_as_float(gt0)), om1 = rbf(1.f - __uint_as_float(gt1));
#pragma unroll
    for (int i = 0; i < 4; ++i) {
      const float dsk = (i & 1) ? bfhi(dk[i >> 1]) : bflo(dk[i >> 1]);
      const float xa = (i & 1) ? bfhi(xe0[i >> 1]) : bflo(xe0[i >> 1]), xb = (i & 1) ? bfhi(xe1[i >> 1]) : bflo(xe1[i >> 1]);
      const float ya = (i & 1) ? bfhi(y0[i >> 1]) : bflo(y0[i >> 1]), yb = (i & 1) ? bfhi(y1[i >> 1]) : bflo(y1[i >> 1]);
      pre0[i] = om0 * fmaf(xa, dsk, ya);
      pre1[i] = om1 * fmaf(xb, dsk, yb);
    }
  }
  KSTAMP(4, 2);
  if (TAIL_VAR == 1) fetch();
  for (unsigned spin = 0;; ++spin) {
    unsigned mx = 0;
#pragma unroll
    for (int i = 0; i < 4; ++i) mx = max(mx, max(in0[i], max(in1[i], in2[i])));
    if (__all(mx != SENT) || spin > SPIN_CAP) break;
    fetch();
  }
  KSTAMP(4, 6);
  st128(lds + L_OS + ((hg + 1) & 3) * 8192 + tid * 16, in0);
  st128(lds + L_OS + ((hg + 2) & 3) * 8192 + tid * 16, in1);
  st128(lds + L_OS + ((hg + 3) & 3) * 8192 + tid * 16, in2);
  STAMP(13);
  BAR_LDS();
  STAMP(14);
  if (w < 7) {
    const F4 b10 = bread(1, 0), b11 = bread(1, 1), b20 = bread(2, 0), b21 = bread(2, 1);
    SBAR();
    omul(1, b10, b11);
    const F4 b30 = bread(3, 0), b31 = bread(3, 1);
    SBAR();
    omul(2, b20, b21); omul(3, b30, b31);
    const float ga = __uint_as_float(gt0), gb = __uint_as_float(gt1);
    float ra[4], rc[4];
#pragma unroll
    for (int i = 0; i < 4; ++i) { ra[i] = fmaf(ga, acc0[i], pre0[i]); rc[i] = fmaf(gb, acc1[i], pre1[i]); }
    const rsrc_t ro = mkrs(P.out, S * D * 2);
    const int oo = ((32 * qb + c15) * D + n0 + 4 * g4) * 2;
    __builtin_amdgcn_raw_buffer_store_b64((u32x2){cvtpk(ra[0], ra[1]), cvtpk(ra[2], ra[3])}, ro, oo, 0, OUT_AUX);
    __builtin_amdgcn_raw_buffer_store_b64((u32x2){cvtpk(rc[0], rc[1]), cvtpk(rc[2], rc[3])}, ro, oo + 16 * D * 2, 0, OUT_AUX);
  }
}

extern "C" void kernel_launch(void* const* d_in, const int* in_sizes, int n_in, void* d_out, int out_size, void* d_ws, size_t ws_size,
                              hipStream_t stream) {
  Params p;
  p.x = (const u16*)d_in[0]; p.wq = (const u16*)d_in[1]; p.wk = (const u16*)d_in[2]; p.wv = (const u16*)d_in[3]; p.wo = (const u16*)d_in[4];
  p.are = (const u16*)d_in[6]; p.aim = (const u16*)d_in[7]; p.bre = (const u16*)d_in[8]; p.bim = (const u16*)d_in[9];
  p.cre = (const u16*)d_in[10]; p.cim = (const u16*)d_in[11]; p.logdt = (const u16*)d_in[12]; p.dsk = (const u16*)d_in[13]; p.gw = (const u16*)d_in[14];
  p.out = (u16*)d_out; p.ws = (char*)d_ws;
  gated_mix_kernel<<<256, 512, 0, stream>>>(p);
}
```
